```python
import torch

def _softmax(x, axis=-1):
    return torch.softmax(x, dim=axis)
def _gelu(x):
    return torch.nn.functional.gelu(x, approximate="tanh")
def _silu(x):
    return torch.nn.functional.silu(x)
def _sigmoid(x):
    return torch.sigmoid(x)
def _layernorm(x, axis=-1, eps=1e-5):
    m = x.mean(dim=axis, keepdim=True)
    v = x.var(dim=axis, keepdim=True, unbiased=False)
    return (x - m) / torch.sqrt(v + eps)
def _scatter_add(tgt, idx, src, axis=0):
    return tgt.clone().index_add_(axis, idx, src)
def _segment_sum(src, seg, n):
    out = torch.zeros((n,) + src.shape[1:], dtype=src.dtype)
    return out.index_add_(0, seg, src)
def _pad(x, pw):
    flat = tuple(v for pair in reversed(pw) for v in pair)
    return torch.nn.functional.pad(x, flat)

kernel_name = 'concat_heads__compressed_kv_cla__spectral_gfnet__prompt'

def setup_inputs():

    _w = (lambda a: (lambda t: t.to(torch.bfloat16) if t.is_floating_point() else t)(torch.as_tensor(a)))
    def _A_setup():
        B, H, Sq, Sk, D, M = 1, 32, 2048, 2048, 128, 576
        Dc = 4 * D
        return {
            "x": _w((lambda *s: torch.randn(*s, dtype=torch.bfloat16) * 3.2)(B, Sq, M)),
            "c_kv": _w((lambda *s: torch.randn(*s, dtype=torch.bfloat16) * 0.05)(B, Sk, Dc)),
            "w_q": _w((lambda *s: torch.randn(*s, dtype=torch.bfloat16) * 3.2)(M, H, D) * (M ** -0.5)),
            "w_uk": _w((lambda *s: torch.randn(*s, dtype=torch.bfloat16) * 3.2)(Dc, H, D) * (Dc ** -0.5)),
            "w_uv": _w((lambda *s: torch.randn(*s, dtype=torch.bfloat16) * 0.05)(Dc, H, D) * (Dc ** -0.5)),
            "w_o": _w((lambda *s: torch.randn(*s, dtype=torch.bfloat16) * 0.05)(H, D, M) * ((H * D) ** -0.5)),
        }
    _A_in = _A_setup()

    def _B_setup():
        B, S, D = 1, 2048, 576
        Df = D // 2 + 1
        return {
            "x": _w((lambda *s: torch.randn(*s, dtype=torch.bfloat16) * 0.05)(B, S, D)),
            "w_r": _w((lambda *s: torch.randn(*s, dtype=torch.bfloat16) * 0.05)(S, Df) * 0.02),
            "w_i": _w((lambda *s: torch.randn(*s, dtype=torch.bfloat16) * 0.05)(S, Df) * 0.02),
        }
    _B_in = _B_setup()

    return {
        "A_x": _A_in["x"],
        "A_c_kv": _A_in["c_kv"],
        "A_w_q": _A_in["w_q"],
        "A_w_uk": _A_in["w_uk"],
        "A_w_uv": _A_in["w_uv"],
        "A_w_o": _A_in["w_o"],
        "B_x": _B_in["x"],
        "B_w_r": _B_in["w_r"],
        "B_w_i": _B_in["w_i"],
    }

def reference(A_x, A_c_kv, A_w_q, A_w_uk, A_w_uv, A_w_o, B_x, B_w_r, B_w_i):
    def _A_ref(x, c_kv, w_q, w_uk, w_uv, w_o):
        B, Sq, M = x.shape[0], x.shape[1], x.shape[2]
        Sk = c_kv.shape[1]
        H, D = w_q.shape[1], w_q.shape[2]
        q = torch.einsum("bqm,mhd->bhqd", x, w_q)
        k = torch.einsum("bkc,chd->bhkd", c_kv, w_uk)
        v = torch.einsum("bkc,chd->bhkd", c_kv, w_uv)
        scores = torch.einsum("bhqd,bhkd->bhqk", q, k) * (D ** -0.5)
        off = Sk - Sq
        qi = torch.arange(Sq)[:, None]
        kj = torch.arange(Sk)[None, :]
        cmask = kj <= (qi + off)
        scores = torch.where(cmask[None, None], scores, -1e9)
        p = _softmax(scores, axis=-1)
        ctx = torch.einsum("bhqk,bhkd->bhqd", p, v)
        return torch.einsum("bhqd,hdm->bqm", ctx, w_o)

    def _B_ref(x, w_r, w_i):
        B, S, D = x.shape
        X = (lambda x, axes=(-2,-1): torch.fft.rfft2(x.float(), dim=axes))(x, axes=(-2, -1))
        W = w_r + 1j * w_i
        X = X * W
        y = (lambda x, s, axes=(-2,-1): torch.fft.irfft2(x, s=s, dim=axes).to(torch.bfloat16))(X, (S, D), axes=(-2, -1))
        return y

    a = _A_ref(A_x, A_c_kv, A_w_q, A_w_uk, A_w_uv, A_w_o)
    b = _B_ref(B_x, B_w_r, B_w_i)
    return torch.concatenate([a, b], axis=-1)
```

```cpp
#include <hip/hip_runtime.h>
#include <hip/hip_bf16.h>
typedef unsigned short u16;
__device__ __forceinline__ float bf2f(u16 b){ return __uint_as_float(((unsigned)b)<<16); }
__device__ __forceinline__ u16 f2bf(float f){ unsigned u=__float_as_uint(f); u += 0x7fffu + ((u>>16)&1u); return (u16)(u>>16); }
constexpr int S=2048,H=32,D=128,M=576,DC=512,DF=289;
__global__ void proj(const u16* A,const u16* W,u16* C,int K){
  int n=blockIdx.x*256+threadIdx.x; int s=blockIdx.y; float acc=0;
  for(int k=0;k<K;++k) acc+=bf2f(A[s*K+k])*bf2f(W[(size_t)k*4096+n]);
  C[(size_t)s*4096+n]=f2bf(acc);
}
__global__ void attn(const u16* Q,const u16* Kk,const u16* V,u16* ctx){
  __shared__ float sc[2048]; __shared__ float qs[128]; __shared__ float red[128];
  int q=blockIdx.x,h=blockIdx.y,t=threadIdx.x;
  qs[t]=bf2f(Q[(size_t)q*4096+h*128+t]); __syncthreads();
  float mx=-1e30f;
  for(int j=t;j<=q;j+=128){ float a=0; for(int d=0;d<128;++d) a+=qs[d]*bf2f(Kk[(size_t)j*4096+h*128+d]); a*=0.08838834764831845f; sc[j]=a; mx=fmaxf(mx,a);}
  red[t]=mx; __syncthreads(); for(int o=64;o>0;o>>=1){ if(t<o) red[t]=fmaxf(red[t],red[t+o]); __syncthreads(); } mx=red[0]; __syncthreads();
  float sum=0; for(int j=t;j<=q;j+=128){ float p=__expf(sc[j]-mx); sc[j]=p; sum+=p; }
  red[t]=sum; __syncthreads(); for(int o=64;o>0;o>>=1){ if(t<o) red[t]+=red[t+o]; __syncthreads(); } sum=red[0];
  float a=0; for(int j=0;j<=q;++j) a+=sc[j]*bf2f(V[(size_t)j*4096+h*128+t]);
  ctx[(size_t)q*4096+h*128+t]=f2bf(a/sum);
}
__global__ void oproj(const u16* ctx,const u16* Wo,u16* out){
  int m=blockIdx.x*64+threadIdx.x; int s=blockIdx.y; float acc=0;
  for(int k=0;k<4096;++k) acc+=bf2f(ctx[(size_t)s*4096+k])*bf2f(Wo[(size_t)k*576+m]);
  out[(size_t)s*1152+m]=f2bf(acc);
}
__global__ void b1(const u16* x,float2* X1){
  int k=blockIdx.x*64+threadIdx.x; int s=blockIdx.y; if(k>=DF) return; float re=0,im=0;
  for(int d=0;d<576;++d){ int r=(d*k)%576; float sn,cs; sincospif(-2.0f*r/576.0f,&sn,&cs); float v=bf2f(x[s*576+d]); re+=v*cs; im+=v*sn; }
  X1[s*DF+k]=make_float2(re,im);
}
__global__ void b2(const float2* X1,const u16* wr,const u16* wi,float2* Y,int inv){
  __shared__ float2 tw[2048];
  for(int i=threadIdx.x;i<2048;i+=64){ float sn,cs; sincospif((inv?2.0f:-2.0f)*i/2048.0f,&sn,&cs); tw[i]=make_float2(cs,sn);} __syncthreads();
  int k=blockIdx.x*64+threadIdx.x; int sp=blockIdx.y; if(k>=DF) return; float re=0,im=0;
  for(int s=0;s<2048;++s){ float2 w=tw[(s*sp)&2047]; float2 v=X1[s*DF+k]; re+=v.x*w.x-v.y*w.y; im+=v.x*w.y+v.y*w.x; }
  if(!inv){ float a=bf2f(wr[sp*DF+k]),b=bf2f(wi[sp*DF+k]); float r2=re*a-im*b,i2=re*b+im*a; re=r2; im=i2; }
  Y[sp*DF+k]=make_float2(re,im);
}
__global__ void b3(const float2* Z,u16* out){
  int d=blockIdx.x*64+threadIdx.x; int s=blockIdx.y; float acc=Z[s*DF].x + ((d&1)?-1.f:1.f)*Z[s*DF+288].x;
  for(int k=1;k<288;++k){ int r=(d*k)%576; float sn,cs; sincospif(2.0f*r/576.0f,&sn,&cs); float2 z=Z[s*DF+k]; acc+=2.f*(z.x*cs-z.y*sn); }
  out[(size_t)s*1152+576+d]=f2bf(acc*(1.0f/(2048.0f*576.0f)));
}
extern "C" void kernel_launch(void* const* d_in,const int* in_sizes,int n_in,void* d_out,int out_size,void* d_ws,size_t ws_size,hipStream_t stream){
  const u16* x=(const u16*)d_in[0],*c=(const u16*)d_in[1],*wq=(const u16*)d_in[2],*wuk=(const u16*)d_in[3],*wuv=(const u16*)d_in[4],*wo=(const u16*)d_in[5];
  const u16* bx=(const u16*)d_in[6],*wr=(const u16*)d_in[7],*wi=(const u16*)d_in[8];
  u16* out=(u16*)d_out; char* ws=(char*)d_ws; size_t T=(size_t)2048*4096*2;
  u16* Q=(u16*)ws,*K=(u16*)(ws+T),*V=(u16*)(ws+2*T),*ctx=(u16*)(ws+3*T);
  float2* X1=(float2*)(ws+4*T),*Y=(float2*)(ws+4*T+(8<<20)),*Z=(float2*)(ws+4*T+(16<<20));
  proj<<<dim3(16,2048),256,0,stream>>>(x,wq,Q,576);
  proj<<<dim3(16,2048),256,0,stream>>>(c,wuk,K,512);
  proj<<<dim3(16,2048),256,0,stream>>>(c,wuv,V,512);
  attn<<<dim3(2048,32),128,0,stream>>>(Q,K,V,ctx);
  oproj<<<dim3(9,2048),64,0,stream>>>(ctx,wo,out);
  b1<<<dim3(5,2048),64,0,stream>>>(bx,X1);
  b2<<<dim3(5,2048),64,0,stream>>>(X1,wr,wi,Y,0);
  b2<<<dim3(5,2048),64,0,stream>>>(Y,wr,wi,Z,1);
  b3<<<dim3(9,2048),64,0,stream>>>(Z,out);
}
```

```cpp
#include <hip/hip_runtime.h>
#include <stdint.h>
#include <utility>
typedef unsigned short u16;
typedef short bf16x8 __attribute__((ext_vector_type(8)));
typedef short s16x4 __attribute__((ext_vector_type(4)));
typedef float f32x2 __attribute__((ext_vector_type(2)));
typedef float f32x4 __attribute__((ext_vector_type(4)));
typedef float f32x16 __attribute__((ext_vector_type(16)));
typedef unsigned u32x4 __attribute__((ext_vector_type(4)));
typedef unsigned u32x2 __attribute__((ext_vector_type(2)));
typedef __bf16 bf16x2_t __attribute__((ext_vector_type(2)));
#define LAS __attribute__((address_space(3)))
#define DI __device__ __forceinline__
#define BAR() __builtin_amdgcn_s_barrier()
#define SCHED() __builtin_amdgcn_sched_barrier(0)
#define WAIT_L0() asm volatile("s_waitcnt lgkmcnt(0)" ::: "memory")
template <int N> DI void wait_v() { asm volatile("s_waitcnt vmcnt(%0)" ::"n"(N) : "memory"); }
DI unsigned cvtpk(float lo, float hi) { f32x2 v = {lo, hi}; bf16x2_t b = __builtin_convertvector(v, bf16x2_t); return __builtin_bit_cast(unsigned, b); }
DI void glds16(const void* g, LAS unsigned char* l) { __builtin_amdgcn_global_load_lds((const unsigned*)g, (LAS unsigned*)l, 16, 0, 0); }
DI bf16x8 lds_b128(const LAS unsigned char* p) { return *(const LAS bf16x8*)p; }
template <int OFF> DI s16x4 lds_tr(unsigned a) { s16x4 r; asm volatile("ds_read_b64_tr_b16 %0, %1 offset:%2" : "=v"(r) : "v"(a), "n"(OFF) : "memory"); return r; }
typedef __amdgpu_buffer_rsrc_t rsrc_t;
DI rsrc_t mk_rsrc(const void* p, unsigned bytes) { return __builtin_amdgcn_make_buffer_rsrc((void*)p, 0, bytes, 0x00020000); }
DI void blds16(rsrc_t r, LAS unsigned char* l, unsigned voff, unsigned soff) { __builtin_amdgcn_raw_ptr_buffer_load_lds(r, (LAS void*)l, 16, voff, soff, 0, 0); }
DI unsigned opaque(unsigned x) { asm volatile("" : "+v"(x)); return x; }

constexpr int SEQ = 2048, NH = 32, HD = 128, DM = 576, DC = 512, NQKV = 4096;
constexpr float QSCALE = 0.08838834764831845f * 1.4426950408889634f;

namespace k1 {
constexpr int NS = 10, SLOT = 16384, NL = 59, NP = 50, NPRO = 8;
struct HT { int kind, kt, rd; };
constexpr HT ht(int i) {
    if (i < 32) { const int t = i >> 2, e = i & 3;
        return e == 0 ? HT{0, t, 4 * t + 1} : e == 1 ? HT{3, t, 4 * t + 1} : e == 2 ? HT{1, t, 4 * t + 2} : HT{4, t, 4 * t + 3}; }
    const int u = (i - 32) / 3, e = (i - 32) % 3;
    return e == 0 ? HT{2, u, 33 + 2 * u} : e == 1 ? HT{5, u, 33 + 2 * u} : HT{6, u, 34 + 2 * u};
}
constexpr int nstores(int p) { return (p >= 30 && p <= 33) ? 4 : 0; }
struct Sched { int issue[NL]; int wait[NP + 1]; int first[NP + 2]; };
constexpr Sched make_sched() {
    Sched s{}; int cnt[NP + 2] = {};
    for (int i = 0; i < NL; ++i) {
        int e = i < NPRO ? 0 : 1;
        if (i >= NS && ht(i - NS).rd + 2 > e) e = ht(i - NS).rd + 2;
        if (i > 0 && s.issue[i - 1] > e) e = s.issue[i - 1];
        while (e > 0 && cnt[e] >= 2) ++e;
        s.issue[i] = e; cnt[e]++;
    }
    int endpos[NL] = {}; int tot = 0, li = 0, prev_last = -1;
    for (int p = 0; p <= NP; ++p) {
        s.first[p] = li;
        tot += nstores(p);
        while (li < NL && s.issue[li] == p) { tot += 2; endpos[li] = tot; ++li; }
        int last = -1; for (int i = 0; i < NL; ++i) if (ht(i).rd <= p + 1) last = i;
        if (last > prev_last) { s.wait[p] = (s.issue[last] <= p) ? tot - endpos[last] : -1000; prev_last = last; } else s.wait[p] = -1;
    }
    s.first[NP + 1] = li;
    return s;
}
constexpr Sched SC = make_sched();
constexpr bool sched_ok() { for (int p = 0; p <= NP; ++p) if (SC.wait[p] < -1 || SC.wait[p] > 63) return false; return SC.first[NP + 1] == NL; }
static_assert(sched_ok(), "schedule");

struct St {
    LAS unsigned char* lds;
    rsrc_t rw[3], ra[2]; unsigned sw, sa[2];
    unsigned voffW[2], voffC[2], voffX[2];
    unsigned ldsw, aoff, boff[2];
    u16 *Kp, *Vt, *Qp; int h, sb;
    f32x4 acc[2][2][4][2];
    bf16x8 At[4][2]; s16x4 B0[2][2][2], B1[2][2][2];
};
template <int I> DI void stage(St& s) {
    constexpr HT h = ht(I); constexpr int slot = I % NS;
    LAS unsigned char* dst = s.lds + slot * SLOT + s.ldsw;
    if constexpr (h.kind <= 2) { const unsigned so = s.sw + h.kt * (64 * NQKV * 2); blds16(s.rw[h.kind], dst, s.voffW[0], so); blds16(s.rw[h.kind], dst + 8192, s.voffW[1], so); }
    else if constexpr (h.kind <= 4) { const unsigned so = s.sa[0] + (h.kind - 3) * (128 * DC * 2) + h.kt * 128; blds16(s.ra[0], dst, s.voffC[0], so); blds16(s.ra[0], dst + 8192, s.voffC[1], so); }
    else { const unsigned so = s.sa[1] + (h.kind - 5) * (128 * DM * 2) + h.kt * 128; blds16(s.ra[1], dst, s.voffX[0], so); blds16(s.ra[1], dst + 8192, s.voffX[1], so); }
}
template <int I> DI void lda(St& s) {
    constexpr int slot = I % NS;
    const unsigned a0 = opaque(s.aoff + slot * SLOT), a1 = opaque((s.aoff ^ 64) + slot * SLOT);
#pragma unroll
    for (int m = 0; m < 4; ++m) { s.At[m][0] = lds_b128((const LAS unsigned char*)a0 + m * 2048); s.At[m][1] = lds_b128((const LAS unsigned char*)a1 + m * 2048); }
}
template <int I> DI void ldb(St& s, s16x4 (&B)[2][2][2]) {
    constexpr int slot = I % NS;
#pragma unroll
    for (int n = 0; n < 2; ++n) {
        const unsigned p = s.boff[n] + slot * SLOT;
        B[n][0][0] = lds_tr<0>(p); B[n][0][1] = lds_tr<1024>(p); B[n][1][0] = lds_tr<8192>(p); B[n][1][1] = lds_tr<9216>(p);
    }
}
template <bool TR, bool ZERO> DI void mma(f32x4 (&acc)[4][2], const bf16x8 (&At)[4][2], const s16x4 (&Bh)[2][2][2]) {
    bf16x8 B[2][2];
#pragma unroll
    for (int n = 0; n < 2; ++n)
#pragma unroll
        for (int k = 0; k < 2; ++k) B[n][k] = __builtin_shufflevector(Bh[n][k][0], Bh[n][k][1], 0, 1, 2, 3, 4, 5, 6, 7);
    __builtin_amdgcn_s_setprio(1);
#pragma unroll
    for (int k = 0; k < 2; ++k)
#pragma unroll
        for (int m = 0; m < 4; ++m)
#pragma unroll
            for (int n = 0; n < 2; ++n) {
                const f32x4 c = (ZERO && k == 0) ? (f32x4){0.f, 0.f, 0.f, 0.f} : acc[m][n];
                acc[m][n] = TR ? __builtin_amdgcn_mfma_f32_16x16x32_bf16(B[n][k], At[m][k], c, 0, 0, 0)
                               : __builtin_amdgcn_mfma_f32_16x16x32_bf16(At[m][k], B[n][k], c, 0, 0, 0);
            }
    __builtin_amdgcn_s_setprio(0);
}
template <bool SCALE> DI void store_t(const f32x4 (&a)[4][2], u16* dst) {
#pragma unroll
    for (int m = 0; m < 4; ++m) {
        f32x4 v0 = a[m][0], v1 = a[m][1];
        if (SCALE) { v0 *= QSCALE; v1 *= QSCALE; }
        u32x4 w = {cvtpk(v0[0], v0[1]), cvtpk(v0[2], v0[3]), cvtpk(v1[0], v1[1]), cvtpk(v1[2], v1[3])};
        *(u32x4*)(dst + (size_t)((m >> 1) * 32 + (m & 1) * 8) * HD) = w;
    }
}
DI void store_v(const f32x4 (&a)[4][2], u16* dst) {
#pragma unroll
    for (int u = 0; u < 2; ++u)
#pragma unroll
        for (int n = 0; n < 2; ++n) {
            const f32x4 v0 = a[2 * u][n], v1 = a[2 * u + 1][n];
            u32x4 w = {cvtpk(v0[0], v0[1]), cvtpk(v0[2], v0[3]), cvtpk(v1[0], v1[1]), cvtpk(v1[2], v1[3])};
            *(u32x4*)(dst + (size_t)n * 16 * SEQ + u * 32) = w;
        }
}
DI u16* dst_t(const St& s, u16* base) {
    const int t = (int)opaque(threadIdx.x), wid = t >> 6, lane = t & 63, wr = wid >> 2, wc = wid & 3, fr = lane & 15, fq = lane >> 4;
    return base + ((size_t)s.h * SEQ + s.sb * 256 + wr * 64 + 16 * (fr >> 3) + (fr & 7)) * HD + wc * 32 + 8 * fq;
}
DI u16* dst_v(const St& s) {
    const int t = (int)opaque(threadIdx.x), wid = t >> 6, lane = t & 63, wr = wid >> 2, wc = wid & 3, fr = lane & 15, fq = lane >> 4;
    return s.Vt + ((size_t)s.h * HD + wc * 32 + fr) * SEQ + s.sb * 256 + wr * 64 + 8 * fq;
}
DI void mk_voffx(St& s) {
    const int tid = (int)opaque(threadIdx.x);
#pragma unroll
    for (int i = 0; i < 2; ++i) { const int b = tid * 16 + i * 8192, r = b >> 7, kc = ((b >> 4) & 7) ^ ((r >> 1) & 7), pr = (r & ~24) | ((r & 8) << 1) | ((r & 16) >> 1);
        s.voffX[i] = (unsigned)(pr * DM * 2 + kc * 16); }
}
template <int A, int B> DI void stage_range(St& s) { if constexpr (A < B) { stage<A>(s); stage_range<A + 1, B>(s); } }
template <int P> DI void phase(St& s) {
    if constexpr (P <= 32) {
        constexpr int t = (P - 1) >> 2, e = (P - 1) & 3;
        if constexpr (e == 0) { ldb<4 * t>(s, s.B0); lda<4 * t + 1>(s); }
        if constexpr (e == 1) ldb<4 * t + 2>(s, s.B1);
        if constexpr (e == 2) lda<4 * t + 3>(s);
    } else {
        constexpr int u = (P - 33) >> 1, e = (P - 33) & 1;
        if constexpr (e == 0) { ldb<32 + 3 * u>(s, s.B0); lda<33 + 3 * u>(s); }
        else lda<34 + 3 * u>(s);
    }
    if constexpr (P == 30) store_t<false>(s.acc[0][0], dst_t(s, s.Kp));
    if constexpr (P == 31) store_v(s.acc[0][1], dst_v(s));
    if constexpr (P == 32) store_t<false>(s.acc[1][0], dst_t(s, s.Kp) + 128 * HD);
    if constexpr (P == 33) store_v(s.acc[1][1], dst_v(s) + 128);
    if constexpr (P == SC.issue[33]) mk_voffx(s);
    stage_range<SC.first[P], SC.first[P + 1]>(s);
    if constexpr (SC.wait[P] >= 0) wait_v<SC.wait[P]>();
    BAR(); WAIT_L0(); SCHED();
    if constexpr (P <= 32) {
        constexpr int t = (P - 1) >> 2, e = (P - 1) & 3;
        if constexpr (e == 0) mma<true, t == 0>(s.acc[0][0], s.At, s.B0);
        if constexpr (e == 1) mma<false, t == 0>(s.acc[0][1], s.At, s.B1);
        if constexpr (e == 2) mma<true, t == 0>(s.acc[1][0], s.At, s.B0);
        if constexpr (e == 3) mma<false, t == 0>(s.acc[1][1], s.At, s.B1);
    } else {
        constexpr int u = (P - 33) >> 1, e = (P - 33) & 1;
        mma<true, u == 0>(s.acc[e][0], s.At, s.B0);
    }
    BAR(); SCHED();
}
template <int... Ps> DI void phases(St& s, std::integer_sequence<int, Ps...>) { (phase<Ps + 1>(s), ...); }
}

__global__ __launch_bounds__(512, 2) void k1_qkv(const u16* __restrict__ x, const u16* __restrict__ c, const u16* __restrict__ wq, const u16* __restrict__ wuk,
                                                 const u16* __restrict__ wuv, u16* __restrict__ Qp, u16* __restrict__ Kp, u16* __restrict__ Vt) {
    using namespace k1;
    extern __shared__ __attribute__((aligned(16))) unsigned char smem[];
    St s; s.lds = (LAS unsigned char*)smem;
    const int tid = threadIdx.x, wid = __builtin_amdgcn_readfirstlane(tid >> 6), lane = tid & 63, wr = wid >> 2, wc = wid & 3, fr = lane & 15, fq = lane >> 4;
    const int bid = blockIdx.x, xcd = bid & 7, idx = bid >> 3, h = xcd * 4 + (idx & 3), sb = idx >> 2;
    s.rw[0] = mk_rsrc(wuk, DC * NQKV * 2); s.rw[1] = mk_rsrc(wuv, DC * NQKV * 2); s.rw[2] = mk_rsrc(wq, DM * NQKV * 2);
    s.ra[0] = mk_rsrc(c, SEQ * DC * 2); s.ra[1] = mk_rsrc(x, SEQ * DM * 2);
    s.sw = h * HD * 2; s.sa[0] = sb * 256 * DC * 2; s.sa[1] = sb * 256 * DM * 2;
    s.Kp = Kp; s.Vt = Vt; s.Qp = Qp; s.h = h; s.sb = sb;
#pragma unroll
    for (int i = 0; i < 2; ++i) {
        const int b = tid * 16 + i * 8192;
        { const int k = b >> 8, u = (b >> 5) & 7, hf = (b >> 4) & 1, f = (k & 3) | (((k >> 3) & 1) << 2); s.voffW[i] = (unsigned)(k * NQKV * 2 + ((u ^ f) * 16 + hf * 8) * 2); }
        { const int r = b >> 7, kc = ((b >> 4) & 7) ^ ((r >> 1) & 7), pr = (r & ~24) | ((r & 8) << 1) | ((r & 16) >> 1);
          s.voffC[i] = (unsigned)(pr * DC * 2 + kc * 16); }
    }
    s.ldsw = wid * 1024;
    const unsigned lbase = (unsigned)(uintptr_t)s.lds;
    s.aoff = lbase + (unsigned)((wr * 64 + fr) * 128 + ((fq ^ ((fr >> 1) & 7)) * 16));
    { const int q = (lane & 15) >> 2, p = lane & 3, f = q | ((fq & 1) << 2);
#pragma unroll
      for (int n = 0; n < 2; ++n) s.boff[n] = lbase + (unsigned)(256 * (8 * fq + q) + 32 * ((2 * wc + n) ^ f) + 8 * p); }
    stage_range<0, NPRO>(s);
    wait_v<SC.wait[0]>();
    BAR();
    if (wr == 1) BAR();
    phases(s, std::make_integer_sequence<int, NP>{});
    if (wr == 0) BAR();
    u16* qd = dst_t(s, s.Qp);
    store_t<true>(s.acc[0][0], qd);
    store_t<true>(s.acc[1][0], qd + 128 * HD);
}
namespace k2 {
constexpr int KBUF = 0, VBUF = 65536, TILE = 32768, XTRA = 131072;
constexpr float THR = 8.0f;
#define K2_PD 4
constexpr int PD = K2_PD;
#define MFMA32(a, b, c) __builtin_amdgcn_mfma_f32_32x32x16_bf16((a), (b), (c), 0, 0, 0)
#define PIN(x) asm volatile("" : "+v"(x))
DI float max2f(float a, float b) { return __builtin_elementwise_maximum(a, b); }
DI float max3f(float a, float b, float c) { return max2f(max2f(a, b), c); }
DI float swap_max(float x) { auto r = __builtin_amdgcn_permlane32_swap(__float_as_uint(x), __float_as_uint(x), false, false); return max2f(__uint_as_float(r[0]), __uint_as_float(r[1])); }
DI float swap_add(float x) { auto r = __builtin_amdgcn_permlane32_swap(__float_as_uint(x), __float_as_uint(x), false, false); return __uint_as_float(r[0]) + __uint_as_float(r[1]); }
DI bf16x8 pack8(const f32x16& s, int o) {
    u32x4 w = {cvtpk(s[o], s[o + 1]), cvtpk(s[o + 2], s[o + 3]), cvtpk(s[o + 4], s[o + 5]), cvtpk(s[o + 6], s[o + 7])};
    return __builtin_bit_cast(bf16x8, w);
}
DI void exp16(f32x16& s, float& l0, float& l1) {
#pragma unroll
    for (int r = 0; r < 16; ++r) s[r] = __builtin_amdgcn_exp2f(s[r]);
#pragma unroll
    for (int r = 0; r < 16; r += 2) { l0 += s[r]; l1 += s[r + 1]; }
}
DI float max16(const f32x16& a) {
    float m0 = max3f(a[0], a[1], a[2]), m1 = max3f(a[3], a[4], a[5]);
    m0 = max3f(m0, a[6], a[7]); m1 = max3f(m1, a[8], a[9]); m0 = max3f(m0, a[10], a[11]); m1 = max3f(m1, a[12], a[13]);
    return max3f(m0, m1, max2f(a[14], a[15]));
}
DI void mask16(f32x16& s, int dq) {
    const float NEG = -__builtin_inff();
#pragma unroll
    for (int r = 0; r < 16; ++r) { const int c = (r & 3) + 8 * (r >> 2); if (c > dq) s[r] = NEG; }
}
struct St {
    LAS unsigned char* lds; rsrc_t rK, rV; unsigned voffK, voffV, ldsw, skb, svb;
    unsigned ka, va;
    bf16x8 qr[8], fr[32]; u32x4 p[4];
    f32x16 o[4], s0, s1, negm;
    float m, l0, l1, e0, e1, mxa, mxb, f; bool pend; int dq;
    unsigned stK, stV, soK, soV;
};
template <int I> DI void ldfrag(St& c) {
    if constexpr (I < 16) c.fr[I] = lds_b128((const LAS unsigned char*)(c.ka ^ (unsigned)((I & 7) << 5)) + (I >> 3) * 8192);
    else if constexpr (I < 32) c.fr[I] = lds_b128((const LAS unsigned char*)(c.va ^ (unsigned)(((I - 16) >> 2) << 5)) + ((I - 16) & 3) * 8192);
}
template <int J> DI void stage1(St& c) {
    if constexpr (J < 4) blds16(c.rK, c.lds + c.stK + J * 4096, c.voffK, c.soK + J * 4096);
    else blds16(c.rV, c.lds + c.stV + (J - 4) * 4096, c.voffV, c.soV + (J - 4) * 65536);
}
template <int PAIR> DI void fin_pair(St& c, const int pbase) {
    c.l0 += c.e0; c.l1 += c.e1; PIN(c.l0); PIN(c.l1);
    unsigned w = cvtpk(c.e0, c.e1); PIN(w);
    c.p[pbase + (PAIR >> 2)][PAIR & 3] = w;
}
template <int G, bool DIAG> DI void gapA(St& c) {
    constexpr int kb = G >> 3, d = G & 7;
    if constexpr (kb == 0) c.s0 = MFMA32(c.fr[G], c.qr[d], d == 0 ? c.negm : c.s0);
    if constexpr (G == 8) { fin_pair<7>(c, 2); }
    if constexpr (kb == 1) c.s1 = MFMA32(c.fr[G], c.qr[d], d == 0 ? c.negm : c.s1);
    ldfrag<G + PD>(c);
    if constexpr (G < 8) {
        if constexpr (G >= 1) fin_pair<G - 1>(c, 2);
        c.e0 = __builtin_amdgcn_exp2f(c.s1[2 * G]); c.e1 = __builtin_amdgcn_exp2f(c.s1[2 * G + 1]); PIN(c.e0); PIN(c.e1);
    } else {
        stage1<G - 8>(c);
        if constexpr (DIAG && G == 11) mask16(c.s0, c.dq);
        if constexpr (G == 12) { c.mxa = max3f(c.s0[0], c.s0[1], c.s0[2]); c.mxb = max3f(c.s0[3], c.s0[4], c.s0[5]); }
        if constexpr (G == 13) { c.mxa = max3f(c.mxa, c.s0[6], c.s0[7]); c.mxb = max3f(c.mxb, c.s0[8], c.s0[9]); }
        if constexpr (G == 14) { c.mxa = max3f(c.mxa, c.s0[10], c.s0[11]); c.mxb = max3f(c.mxb, c.s0[12], c.s0[13]); }
        if constexpr (G == 15) { c.mxa = max3f(c.mxa, c.s0[14], c.s0[15]); }
        if constexpr (G >= 12) { PIN(c.mxa); PIN(c.mxb); }
    }
    SCHED();
}
template <int G, bool DIAG> DI void gapB(St& c) {
    constexpr int ks = G >> 2, db = G & 3;
    c.o[db] = MFMA32(c.fr[16 + G], __builtin_bit_cast(bf16x8, c.p[ks]), c.o[db]);
    ldfrag<16 + G + PD>(c);
    if constexpr (DIAG && G == 1) mask16(c.s1, c.dq - 32);
    if constexpr (G == 2) { c.mxa = max3f(c.mxa, c.s1[0], c.s1[1]); c.mxb = max3f(c.mxb, c.s1[2], c.s1[3]); c.mxa = max3f(c.mxa, c.s1[4], c.s1[5]); c.mxb = max3f(c.mxb, c.s1[6], c.s1[7]); }
    if constexpr (G == 3) { c.mxa = max3f(c.mxa, c.s1[8], c.s1[9]); c.mxb = max3f(c.mxb, c.s1[10], c.s1[11]); c.mxa = max3f(c.mxa, c.s1[12], c.s1[13]); c.mxb = max3f(c.mxb, c.s1[14], c.s1[15]);
                            c.mxa = max2f(c.mxa, c.mxb); }
    if constexpr (G == 2 || G == 3) { PIN(c.mxa); PIN(c.mxb); }
    if constexpr (G >= 8) fin_pair<G - 8>(c, 0);
    if constexpr (G >= 7 && G <= 14) { c.e0 = __builtin_amdgcn_exp2f(c.s0[2 * (G - 7)]); c.e1 = __builtin_amdgcn_exp2f(c.s0[2 * (G - 7) + 1]); PIN(c.e0); PIN(c.e1); }
    SCHED();
}
template <bool DIAG, int... Gs> DI void phaseA(St& c, std::integer_sequence<int, Gs...>) { (gapA<Gs, DIAG>(c), ...); }
template <bool DIAG, int O, int... Gs> DI void phaseB(St& c, std::integer_sequence<int, Gs...>) { (gapB<Gs + O, DIAG>(c), ...); }
template <int... Is> DI void ldfirst(St& c, std::integer_sequence<int, Is...>) { (ldfrag<Is>(c), ...); }
template <bool DIAG> DI void step(St& c) {
    ldfirst(c, std::make_integer_sequence<int, PD>{});
    SCHED();
    phaseA<DIAG>(c, std::make_integer_sequence<int, 16>{});
    phaseB<DIAG, 0>(c, std::make_integer_sequence<int, 4>{});
    c.pend = false;
    if (__builtin_expect(__any(c.mxa > THR), 0)) {
        const float dl = max2f(swap_max(c.mxa), 0.f);
        c.m += dl; c.f = __builtin_amdgcn_exp2f(-dl); c.l0 *= c.f; c.l1 *= c.f; c.pend = true;
#pragma unroll
        for (int r = 0; r < 16; ++r) { c.s0[r] -= dl; c.s1[r] -= dl; c.negm[r] = -c.m; }
    }
    phaseB<DIAG, 4>(c, std::make_integer_sequence<int, 12>{});
    if (__builtin_expect(c.pend, 0)) {
#pragma unroll
        for (int b = 0; b < 4; ++b)
#pragma unroll
            for (int r = 0; r < 16; ++r) c.o[b][r] *= c.f;
    }
}
}

#define STAMP() do {} while (0)
__global__ __launch_bounds__(512, 2) void k2_attn(const u16* __restrict__ Qp, const u16* __restrict__ Kp, const u16* __restrict__ Vt, u16* __restrict__ ctx, unsigned long long* stamps) {
    int nst = 0; STAMP();
    using namespace k2;
    extern __shared__ __attribute__((aligned(16))) unsigned char smem[];
    St c; c.lds = (LAS unsigned char*)smem; LAS unsigned char* lds = c.lds;
    const int tid = threadIdx.x, wid = __builtin_amdgcn_readfirstlane(tid >> 6), lane = tid & 63, wq = wid & 3, grp = wid >> 2, r32 = lane & 31, hi = lane >> 5;
    const int bid = blockIdx.x, xcd = bid & 7, idx = bid >> 3, h = xcd * 4 + (idx & 3), pr = idx >> 2;
    c.rK = mk_rsrc(Kp, NH * SEQ * HD * 2); c.rV = mk_rsrc(Vt, NH * SEQ * HD * 2);
    const unsigned hb = (unsigned)h * (SEQ * HD * 2);
    const int a0 = wid & 3, jb = (wid >> 2) * 4, srow = 4 * a0 + (lane >> 4), sch = (lane & 15) ^ srow;
    c.voffK = (unsigned)(srow * 256 + sch * 16); c.voffV = (unsigned)(srow * 4096 + sch * 16);
    const unsigned ldsw = (unsigned)((16 * jb + 4 * a0) * 256);
    const unsigned skb = hb + (unsigned)(16 * jb * 256), svb = hb + (unsigned)(16 * jb * 4096);
    const unsigned lbase = (unsigned)(uintptr_t)lds;
    const unsigned kaddr = lbase + KBUF + (unsigned)((64 * grp + r32) * 256 + ((hi ^ (r32 & 15)) << 4));
    const unsigned vaddr = lbase + VBUF + (unsigned)(r32 * 256 + (((8 * grp + hi) ^ (r32 & 15)) << 4));
    c.dq = 32 * wq + r32 - 64 * grp - 4 * hi;
#define SETSTAGE(par_, tk_, tv_) do { c.stK = KBUF + ((par_) ^ 1) * TILE + ldsw; c.soK = skb + (unsigned)(tk_) * 32768u; c.stV = VBUF + (par_) * TILE + ldsw; c.soV = svb + (unsigned)(tv_) * 256u; } while (0)
#define LOADQ(qb_) do { const u16* qp_ = Qp + ((size_t)h * SEQ + (qb_) * 128 + wq * 32 + r32) * HD + 8 * hi; \
        _Pragma("unroll") for (int d_ = 0; d_ < 8; ++d_) c.qr[d_] = *(const bf16x8*)(qp_ + 16 * d_); } while (0)
#define PVSTEP(ks_, vb_) do { _Pragma("unroll") for (int b_ = 0; b_ < 4; ++b_) { const bf16x8 f_ = lds_b128((const LAS unsigned char*)((vb_) ^ (unsigned)((ks_) << 5)) + b_ * 8192); c.o[b_] = MFMA32(f_, __builtin_bit_cast(bf16x8, c.p[ks_]), c.o[b_]); } } while (0)
    f32x16 zero16;
#pragma unroll
    for (int r = 0; r < 16; ++r) zero16[r] = 0.f;
    int step_ = 0;
    SETSTAGE(1, 0, 0); stage1<0>(c); stage1<1>(c); stage1<2>(c); stage1<3>(c);
    LOADQ(15 - pr);
    wait_v<0>(); BAR(); STAMP();
    for (int blk = 0; blk < 2; ++blk) {
        const int qb = blk == 0 ? 15 - pr : pr, T = qb + 1;
        c.l0 = 0.f; c.l1 = 0.f;
#pragma unroll
        for (int b = 0; b < 4; ++b) c.o[b] = zero16;
        { const int par = step_ & 1;
          SETSTAGE(par, T > 1 ? 1 : 0, 0);
          stage1<0>(c); stage1<1>(c); stage1<2>(c); stage1<3>(c); stage1<4>(c); stage1<5>(c); stage1<6>(c); stage1<7>(c);
          c.ka = kaddr + par * TILE;
          ldfirst(c, std::make_integer_sequence<int, 16>{});
#pragma unroll
          for (int d = 0; d < 8; ++d) c.s0 = MFMA32(c.fr[d], c.qr[d], d == 0 ? zero16 : c.s0);
#pragma unroll
          for (int d = 0; d < 8; ++d) c.s1 = MFMA32(c.fr[8 + d], c.qr[d], d == 0 ? zero16 : c.s1);
          if (T == 1) { mask16(c.s0, c.dq); mask16(c.s1, c.dq - 32); }
          c.m = max2f(swap_max(max2f(max16(c.s0), max16(c.s1))), -1e30f);
#pragma unroll
          for (int r = 0; r < 16; ++r) { c.s0[r] -= c.m; c.s1[r] -= c.m; c.negm[r] = -c.m; }
          exp16(c.s0, c.l0, c.l1); c.p[0] = __builtin_bit_cast(u32x4, pack8(c.s0, 0)); c.p[1] = __builtin_bit_cast(u32x4, pack8(c.s0, 8));
          wait_v<0>(); BAR(); ++step_; STAMP(); }
        for (int t = 1; t < T - 1; ++t) {
            const int par = step_ & 1;
            SETSTAGE(par, t + 1, t);
            c.ka = kaddr + par * TILE; c.va = vaddr + (par ^ 1) * TILE;
            step<false>(c);
            wait_v<0>(); BAR();
            ++step_; STAMP();
        }
        if (T > 1) {
            const int par = step_ & 1;
            SETSTAGE(par, 0, T - 1);
            c.ka = kaddr + par * TILE; c.va = vaddr + (par ^ 1) * TILE;
            step<true>(c);
            if (blk == 0) LOADQ(pr);
            wait_v<0>(); BAR(); ++step_; STAMP();
        }
        { const unsigned vb = vaddr + ((step_ - 1) & 1) * TILE;
          PVSTEP(0, vb); PVSTEP(1, vb);
          exp16(c.s1, c.l0, c.l1); c.p[2] = __builtin_bit_cast(u32x4, pack8(c.s1, 0)); c.p[3] = __builtin_bit_cast(u32x4, pack8(c.s1, 8));
          PVSTEP(2, vb); PVSTEP(3, vb); }
        STAMP();
        { const float l = swap_add(c.l0 + c.l1), m = c.m;
          LAS unsigned char* xw = lds + (grp == 0 ? KBUF + ((step_ - 1) & 1) * TILE : VBUF + (step_ & 1) * TILE) + wq * 8192 + lane * 16;
          LAS unsigned char* xr = lds + (grp == 1 ? KBUF + ((step_ - 1) & 1) * TILE : VBUF + (step_ & 1) * TILE) + wq * 8192 + lane * 16;
          const f32x16 sa = grp == 0 ? c.o[2] : c.o[0], sb = grp == 0 ? c.o[3] : c.o[1];
#pragma unroll
          for (int i = 0; i < 4; ++i) { *(LAS f32x4*)(xw + i * 1024) = (f32x4){sa[4 * i], sa[4 * i + 1], sa[4 * i + 2], sa[4 * i + 3]};
                                        *(LAS f32x4*)(xw + 4096 + i * 1024) = (f32x4){sb[4 * i], sb[4 * i + 1], sb[4 * i + 2], sb[4 * i + 3]}; }
          *(LAS f32x2*)(lds + XTRA + wid * 512 + lane * 8) = (f32x2){m, l};
          WAIT_L0(); BAR();
          f32x4 xa[4], xb[4];
#pragma unroll
          for (int i = 0; i < 4; ++i) { xa[i] = *(const LAS f32x4*)(xr + i * 1024); xb[i] = *(const LAS f32x4*)(xr + 4096 + i * 1024); }
          const f32x2 ml = *(const LAS f32x2*)(lds + XTRA + (wid ^ 4) * 512 + lane * 8);
          WAIT_L0(); BAR();
          const float mt = max2f(m, ml[0]), w0 = __builtin_amdgcn_exp2f(m - mt), w1 = __builtin_amdgcn_exp2f(ml[0] - mt);
          const float rl = 1.0f / (w0 * l + w1 * ml[1]), c0 = w0 * rl, c1 = w1 * rl;
          const f32x16 ka = grp == 0 ? c.o[0] : c.o[2], kb = grp == 0 ? c.o[1] : c.o[3];
          u16* dst = ctx + (size_t)(qb * 128 + wq * 32 + r32) * NQKV + h * HD + grp * 64 + 8 * hi;
#pragma unroll
          for (int u = 0; u < 2; ++u) {
              float v[8], w[8];
#pragma unroll
              for (int j = 0; j < 8; ++j) { v[j] = ka[8 * u + j] * c0 + xa[2 * u + (j >> 2)][j & 3] * c1; w[j] = kb[8 * u + j] * c0 + xb[2 * u + (j >> 2)][j & 3] * c1; }
              *(u32x4*)(dst + 16 * u) = (u32x4){cvtpk(v[0], v[1]), cvtpk(v[2], v[3]), cvtpk(v[4], v[5]), cvtpk(v[6], v[7])};
              *(u32x4*)(dst + 32 + 16 * u) = (u32x4){cvtpk(w[0], w[1]), cvtpk(w[2], w[3]), cvtpk(w[4], w[5]), cvtpk(w[6], w[7])};
          } }
        STAMP();
    }
#undef SETSTAGE
#undef LOADQ
#undef PVSTEP
}
namespace k3 {
constexpr int ABYTES = 16384, BBYTES = 18432, STG = ABYTES + BBYTES, NKT = 16, NSTG = 4;
constexpr int SLABQ = 1024;
typedef __attribute__((address_space(1))) unsigned gu32;
struct St {
    LAS unsigned char* lds; rsrc_t rA, rB; unsigned voffA[2], voffB[3], sa, sb, ldsw; bool extra;
    unsigned aoff, boff;
    f32x4 acc[2][9];
    bf16x8 af[2][2]; s16x4 bf[2][9][2];
};
template <int T> DI void stage(St& s) {
    if constexpr (T < NKT) {
        LAS unsigned char* d = s.lds + (T % NSTG) * STG + s.ldsw;
        blds16(s.rA, d, s.voffA[0], s.sa + T * 128); blds16(s.rA, d + 8192, s.voffA[1], s.sa + T * 128);
        blds16(s.rB, d + ABYTES, s.voffB[0], s.sb + T * (64 * DM * 2)); blds16(s.rB, d + ABYTES + 8192, s.voffB[1], s.sb + T * (64 * DM * 2));
        if (s.extra) blds16(s.rB, d + ABYTES + 16384, s.voffB[2], s.sb + T * (64 * DM * 2));
    }
}
template <int T> DI void ldfr(St& s) {
    if constexpr (T < NKT) {
        constexpr int X = T & 1;
        const unsigned a = opaque(s.aoff + (T % NSTG) * STG), b = s.boff + (T % NSTG) * STG;
        s.af[X][0] = lds_b128((const LAS unsigned char*)a); s.af[X][1] = lds_b128((const LAS unsigned char*)a + 2048);
#define K3_LB(c) s.bf[X][c][0] = lds_tr<(c) * 32>(b); s.bf[X][c][1] = lds_tr<(c) * 32 + 16 * 288>(b)
        K3_LB(0); K3_LB(1); K3_LB(2); K3_LB(3); K3_LB(4); K3_LB(5); K3_LB(6); K3_LB(7); K3_LB(8);
#undef K3_LB
    }
}
template <int N> DI void waitv2(bool extra) { if (extra) wait_v<N * 5>(); else wait_v<N * 4>(); }
template <int T> DI void iter(St& s) {
    constexpr int X = T & 1;
    if constexpr (T + 1 < NKT) { waitv2<(T + 2 < NKT) ? 1 : 0>(s.extra); }
    WAIT_L0();
    BAR(); SCHED();
    stage<T + 3>(s);
    ldfr<T + 1>(s);
    SCHED();
#pragma unroll
    for (int c = 0; c < 9; ++c) {
        const bf16x8 b = __builtin_shufflevector(s.bf[X][c][0], s.bf[X][c][1], 0, 1, 2, 3, 4, 5, 6, 7);
#pragma unroll
        for (int m = 0; m < 2; ++m) s.acc[m][c] = __builtin_amdgcn_mfma_f32_16x16x32_bf16(b, s.af[X][m], T == 0 ? (f32x4){0.f, 0.f, 0.f, 0.f} : s.acc[m][c], 0, 0, 0);
    }
    SCHED();
}
template <int... Ts> DI void iters(St& s, std::integer_sequence<int, Ts...>) { (iter<Ts>(s), ...); }
}

__global__ __launch_bounds__(512, 2) void k3_oproj(const u16* __restrict__ ctx, const u16* __restrict__ wo, u16* __restrict__ out, float* __restrict__ slab, unsigned* __restrict__ cnt) {
    using namespace k3;
    extern __shared__ __attribute__((aligned(16))) unsigned char smem[];
    St s; s.lds = (LAS unsigned char*)smem;
    const int tid = threadIdx.x, wid = __builtin_amdgcn_readfirstlane(tid >> 6), lane = tid & 63, wm = wid & 3, kh = wid >> 2, fr = lane & 15, fq = lane >> 4;
    const int bid = blockIdx.x, xcd = bid & 7, idx = bid >> 3;
    const int sk = idx & 3, nt = (idx >> 2) & 3, mt = xcd * 2 + (idx >> 4), tile = mt * 4 + nt;
    s.rA = mk_rsrc(ctx, SEQ * NQKV * 2); s.rB = mk_rsrc(wo, NQKV * DM * 2);
    s.sa = (unsigned)(mt * 128 * NQKV * 2 + sk * 1024 * 2); s.sb = (unsigned)(sk * 1024 * DM * 2 + nt * 144 * 2);
    s.extra = wid < 2; s.ldsw = wid * 1024;
#pragma unroll
    for (int i = 0; i < 2; ++i) { const int b = tid * 16 + i * 8192, r = b >> 7, kc = ((b >> 4) & 7) ^ ((r >> 1) & 7); s.voffA[i] = (unsigned)(r * NQKV * 2 + kc * 16); }
#pragma unroll
    for (int i = 0; i < 3; ++i) { const int b = tid * 16 + i * 8192, rho = (b / 288) & 63, ch = (b % 288) >> 4;
        const int r5 = rho & 31, pos = (rho & 32) + 8 * ((r5 >> 2) & 3) + 4 * (r5 >> 4) + (r5 & 3);
        const int d = (pos & ~15) + 8 * ((pos >> 2) & 1) + 4 * ((pos >> 3) & 1) + (pos & 3);
        s.voffB[i] = (unsigned)(d * DM * 2 + ch * 16); }
    const unsigned lbase = (unsigned)(uintptr_t)s.lds;
    s.aoff = lbase + (unsigned)((wm * 32 + fr) * 128 + (((4 * kh + fq) ^ ((fr >> 1) & 7)) * 16));
    s.boff = lbase + ABYTES + (unsigned)((32 * kh + 4 * fq + ((lane & 15) >> 2)) * 288 + 8 * (lane & 3));
    stage<0>(s); stage<1>(s); stage<2>(s);
    waitv2<2>(s.extra); BAR();
    ldfr<0>(s);
    iters(s, std::make_integer_sequence<int, NKT>{});
    BAR();
    { LAS unsigned char* xw = s.lds + wid * 9216 + lane * 16; const LAS unsigned char* xr = s.lds + (wid ^ 4) * 9216 + lane * 16;
#pragma unroll
      for (int c = 0; c < 9; ++c) *(LAS f32x4*)(xw + c * 1024) = kh == 0 ? s.acc[1][c] : s.acc[0][c];
      WAIT_L0(); BAR();
#pragma unroll
      for (int c = 0; c < 9; ++c) { const f32x4 o = *(const LAS f32x4*)(xr + c * 1024); s.acc[0][c] = (kh == 0 ? s.acc[0][c] : s.acc[1][c]) + o; } }
    const rsrc_t rS = mk_rsrc(slab, 64 * 4 * 4 * 8 * 3 * SLABQ);
    const unsigned sbase = (unsigned)(tile * (4 * 4 * 8 * 3 * SLABQ) + wid * (3 * SLABQ));
#pragma unroll
    for (int c = 0; c < 9; ++c) {
        const int own = c < 8 ? (c & 3) : wm, slot = c >> 2;
        if (own != sk) __builtin_amdgcn_raw_buffer_store_b128(__builtin_bit_cast(u32x4, s.acc[0][c]), rS, lane * 16, sbase + (unsigned)((own * 4 + sk) * (8 * 3 * SLABQ) + slot * SLABQ), 16);
    }
    wait_v<0>();
    if (lane < 4 && lane != sk) __hip_atomic_fetch_add((gu32*)(cnt + (tile * 4 + lane) * 64), 1u, __ATOMIC_RELAXED, __HIP_MEMORY_SCOPE_AGENT);
    if (wid == 0) {
        unsigned spins = 0;
        while (__builtin_amdgcn_readfirstlane(__hip_atomic_load((gu32*)(cnt + (tile * 4 + sk) * 64), __ATOMIC_RELAXED, __HIP_MEMORY_SCOPE_AGENT)) < 24u) {
            if (++spins > (1u << 22)) break;
            __builtin_amdgcn_s_sleep(2);
        }
    }
    asm volatile("" ::: "memory"); BAR(); asm volatile("" ::: "memory");
    { f32x4 part[3][3];
      const bool has3 = wm == sk;
#pragma unroll
      for (int j = 0; j < 3; ++j) { const int src = (sk + 1 + j) & 3; const unsigned so = sbase + (unsigned)((sk * 4 + src) * (8 * 3 * SLABQ));
          part[j][0] = __builtin_bit_cast(f32x4, __builtin_amdgcn_raw_buffer_load_b128(rS, lane * 16, so, 16));
          part[j][1] = __builtin_bit_cast(f32x4, __builtin_amdgcn_raw_buffer_load_b128(rS, lane * 16, so + SLABQ, 16));
          if (has3) part[j][2] = __builtin_bit_cast(f32x4, __builtin_amdgcn_raw_buffer_load_b128(rS, lane * 16, so + 2 * SLABQ, 16)); }
      f32x4 mine[3];
      mine[0] = sk == 0 ? s.acc[0][0] : sk == 1 ? s.acc[0][1] : sk == 2 ? s.acc[0][2] : s.acc[0][3];
      mine[1] = sk == 0 ? s.acc[0][4] : sk == 1 ? s.acc[0][5] : sk == 2 ? s.acc[0][6] : s.acc[0][7];
      mine[2] = s.acc[0][8];
      u16* op = out + (size_t)(mt * 128 + wm * 32 + kh * 16 + fr) * (2 * DM) + nt * 144 + 4 * fq;
#pragma unroll
      for (int q = 0; q < 3; ++q) if (q < 2 || has3) {
          f32x4 v[4];
#pragma unroll
          for (int j = 0; j < 3; ++j) v[j] = part[j][q];
          const f32x4 r = (mine[q] + v[0]) + (v[1] + v[2]);
          const int c = q < 2 ? sk + 4 * q : 8;
          *(u32x2*)(op + 16 * c) = (u32x2){cvtpk(r[0], r[1]), cvtpk(r[2], r[3])};
      } }
}
namespace fb {
typedef f32x2 cf;
constexpr int KP = 304;
DI cf cmul(cf a, cf b) { return (cf){a.x * b.x - a.y * b.y, a.x * b.y + a.y * b.x}; }
DI cf cmulc(cf a, cf b) { return (cf){a.x * b.x + a.y * b.y, a.y * b.x - a.x * b.y}; }
DI cf conj(cf a) { return (cf){a.x, -a.y}; }
template <int S> DI cf muli(cf a) { return S > 0 ? (cf){-a.y, a.x} : (cf){a.y, -a.x}; }
DI cf twid(float rev) { return (cf){__builtin_amdgcn_cosf(rev), __builtin_amdgcn_sinf(rev)}; }
template <int S> DI void dft2(cf& a, cf& b) { const cf t = a - b; a = a + b; b = t; }
template <int S> DI void dft4(cf& a, cf& b, cf& c, cf& d) {
    const cf s0 = a + c, d0 = a - c, s1 = b + d, d1 = muli<S>(b - d);
    a = s0 + s1; c = s0 - s1; b = d0 + d1; d = d0 - d1;
}
template <int S> DI void dft8(cf (&x)[8]) {
    constexpr float R = 0.70710678118654752f;
    dft4<S>(x[0], x[2], x[4], x[6]); dft4<S>(x[1], x[3], x[5], x[7]);
    const cf o1 = (x[3] + muli<S>(x[3])) * R, o2 = muli<S>(x[5]), o3 = (muli<S>(x[7]) - x[7]) * R, o0 = x[1];
    const cf e0 = x[0], e1 = x[2], e2 = x[4], e3 = x[6];
    x[0] = e0 + o0; x[4] = e0 - o0; x[1] = e1 + o1; x[5] = e1 - o1; x[2] = e2 + o2; x[6] = e2 - o2; x[3] = e3 + o3; x[7] = e3 - o3;
}
template <int S> DI void dft3(cf& a, cf& b, cf& c) {
    constexpr float H = 0.86602540378443865f;
    const cf s = b + c, d = muli<S>(b - c) * H, m = a - s * 0.5f;
    a = a + s; b = m + d; c = m - d;
}
template <int S> DI void dft9(cf (&x)[9]) {
    constexpr float C1 = 0.76604444311897801f, S1 = 0.64278760968653933f, C2 = 0.17364817766693035f, S2 = 0.98480775301220806f, C4 = -0.93969262078590838f, S4 = 0.34202014332566873f;
    dft3<S>(x[0], x[3], x[6]); dft3<S>(x[1], x[4], x[7]); dft3<S>(x[2], x[5], x[8]);
    const cf w1 = {C1, S * S1}, w2 = {C2, S * S2}, w4 = {C4, S * S4};
    x[4] = cmul(x[4], w1); x[5] = cmul(x[5], w2); x[7] = cmul(x[7], w2); x[8] = cmul(x[8], w4);
    dft3<S>(x[0], x[1], x[2]); dft3<S>(x[3], x[4], x[5]); dft3<S>(x[6], x[7], x[8]);
    cf t;
    t = x[1]; x[1] = x[3]; x[3] = t; t = x[2]; x[2] = x[6]; x[6] = t; t = x[5]; x[5] = x[7]; x[7] = t;
}
template <int S> DI void dft16(cf (&x)[16]) {
    constexpr float C1 = 0.92387953251128674f, S1 = 0.38268343236508977f, R = 0.70710678118654752f;
#pragma unroll
    for (int n2 = 0; n2 < 4; ++n2) dft4<S>(x[n2], x[4 + n2], x[8 + n2], x[12 + n2]);
    const cf w1 = {C1, S * S1}, w2 = {R, S * R}, w3 = {S1, S * C1}, w6 = {-R, S * R}, w9 = {-C1, -S * S1};
    x[5] = cmul(x[5], w1); x[6] = cmul(x[6], w2); x[7] = cmul(x[7], w3);
    x[9] = cmul(x[9], w2); x[10] = muli<S>(x[10]); x[11] = cmul(x[11], w6);
    x[13] = cmul(x[13], w3); x[14] = cmul(x[14], w6); x[15] = cmul(x[15], w9);
#pragma unroll
    for (int k1 = 0; k1 < 4; ++k1) dft4<S>(x[4 * k1], x[4 * k1 + 1], x[4 * k1 + 2], x[4 * k1 + 3]);
#pragma unroll
    for (int a = 0; a < 4; ++a)
#pragma unroll
        for (int b = a + 1; b < 4; ++b) { const cf t = x[4 * a + b]; x[4 * a + b] = x[4 * b + a]; x[4 * b + a] = t; }
}
constexpr int SEQP = 648;
DI float bfl(unsigned w) { return __uint_as_float(w << 16); }
DI float bfh(unsigned w) { return __uint_as_float(w & 0xffff0000u); }
#define LBAR() do { WAIT_L0(); BAR(); } while (0)
template <int S> DI void fft576x4(LAS cf* L, int tid) {
    if (tid < 256) {
        const int sq = tid >> 6, bc = tid & 63; LAS cf* p = L + sq * SEQP + bc; cf x[9];
#pragma unroll
        for (int a = 0; a < 9; ++a) x[a] = p[72 * a];
        dft9<S>(x);
#pragma unroll
        for (int k = 1; k < 9; ++k) x[k] = cmul(x[k], twid((float)(S * bc * k) * (1.0f / 576.0f)));
#pragma unroll
        for (int a = 0; a < 9; ++a) p[72 * a] = x[a];
    }
    LBAR();
    if (tid < 288) {
        const int sq = tid / 72, r = tid - sq * 72, k1 = r >> 3, c = r & 7; LAS cf* p = L + sq * SEQP + 72 * k1 + c; cf x[8];
#pragma unroll
        for (int b = 0; b < 8; ++b) x[b] = p[8 * b];
        dft8<S>(x);
#pragma unroll
        for (int k = 1; k < 8; ++k) x[k] = cmul(x[k], twid((float)(S * c * k) * (1.0f / 64.0f)));
#pragma unroll
        for (int b = 0; b < 8; ++b) p[8 * b] = x[b];
    }
    LBAR();
    if (tid < 288) {
        const int sq = tid / 72, r = tid - sq * 72, k1 = r >> 3, k2 = r & 7; LAS cf* p = L + sq * SEQP + 72 * k1 + 8 * k2; cf x[8];
#pragma unroll
        for (int c = 0; c < 8; ++c) x[c] = p[c];
        dft8<S>(x);
#pragma unroll
        for (int c = 0; c < 8; ++c) p[c] = x[c];
    }
    LBAR();
}
DI int pos_in(int d) { return 72 * (d >> 6) + (d & 63); }
DI int pos_out(int k) { const int k3 = k / 72, r = k - 72 * k3, k2 = r / 9, k1 = r - 9 * k2; return 72 * k1 + 8 * k2 + k3; }

DI void b1(const u16* __restrict__ x, cf* __restrict__ T1, LAS cf* L, int s0, int tid) {
    if (tid < 288) {
        unsigned w[8];
#pragma unroll
        for (int m = 0; m < 8; ++m) w[m] = *(const unsigned*)(x + (size_t)(s0 + 256 * m) * DM + 2 * tid);
        cf v[8];
#pragma unroll
        for (int m = 0; m < 8; ++m) v[m] = (cf){bfl(w[m]), bfh(w[m])};
        constexpr float R = 0.70710678118654752f;
        const cf a0 = v[0] + v[4], a1 = v[0] - v[4], a2 = v[2] + v[6], a3 = v[2] - v[6], b0 = v[1] + v[5], b1_ = v[1] - v[5], b2 = v[3] + v[7], b3 = v[3] - v[7];
        const cf g0 = (a0 + a2) + (b0 + b2), g4 = (a0 + a2) - (b0 + b2);
        const cf g2r = a0 - a2, g2i = b2 - b0;
        const cf p = (b1_ - b3) * R, q = (b1_ + b3) * R;
        const cf g1r = a1 + p, g1i = -(a3 + q), g3r = a1 - p, g3i = a3 - q;
        const int p0 = pos_in(2 * tid);
        *(LAS f32x4*)(L + 0 * SEQP + p0) = (f32x4){g0.x, g4.x, g0.y, g4.y};
        *(LAS f32x4*)(L + 1 * SEQP + p0) = (f32x4){g1r.x, g1i.x, g1r.y, g1i.y};
        *(LAS f32x4*)(L + 2 * SEQP + p0) = (f32x4){g2r.x, g2i.x, g2r.y, g2i.y};
        *(LAS f32x4*)(L + 3 * SEQP + p0) = (f32x4){g3r.x, g3i.x, g3r.y, g3i.y};
    }
    LBAR();
    fft576x4<-1>(L, tid);
    if (tid < 289) {
        const int k = tid, pa = pos_out(k), pb = pos_out(k == 0 ? 0 : 576 - k);
        cf t[8];
        { const cf a = L[pa], b = conj(L[pb]); t[0] = (a + b) * 0.5f; t[4] = muli<-1>(a - b) * 0.5f; }
#pragma unroll
        for (int j = 1; j < 4; ++j) { t[j] = L[j * SEQP + pa]; t[8 - j] = conj(L[j * SEQP + pb]); }
        cf* o = T1 + (size_t)s0 * KP + k;
        o[0] = t[0];
#pragma unroll
        for (int j = 1; j < 8; ++j) o[(size_t)j * 256 * KP] = cmul(t[j], twid((float)(-s0 * j) * (1.0f / 2048.0f)));
    }
}
DI void b2(const cf* __restrict__ T1, cf* __restrict__ T2, const u16* __restrict__ wr, const u16* __restrict__ wi, LAS cf* L, int j, int kg, int t) {
    const int c = t & 15, a = t >> 4, k = 16 * kg + c; const bool ok = k < 289; const int kk = ok ? k : 288;
    cf x[16]; u16 w_r[16], w_i[16];
    const cf* ip = T1 + ((size_t)j * 256 + a) * KP + kk;
#pragma unroll
    for (int b = 0; b < 16; ++b) x[b] = ip[(size_t)16 * b * KP];
#pragma unroll
    for (int q = 0; q < 16; ++q) { const size_t o = (size_t)(8 * (a + 16 * q) + j) * 289 + kk; w_r[q] = wr[o]; w_i[q] = wi[o]; }
    cf tw[16];
#pragma unroll
    for (int p = 1; p < 16; ++p) tw[p] = twid((float)(a * p) * (1.0f / 256.0f));
    dft16<-1>(x);
#pragma unroll
    for (int p = 1; p < 16; ++p) x[p] = cmulc(x[p], tw[p]);
#pragma unroll
    for (int p = 0; p < 16; ++p) L[(a * 17 + p) * 16 + c] = x[p];
    LBAR();
#pragma unroll
    for (int i = 0; i < 16; ++i) x[i] = L[(i * 17 + a) * 16 + c];
    dft16<-1>(x);
#pragma unroll
    for (int q = 0; q < 16; ++q) x[q] = cmul(x[q], (cf){__uint_as_float((unsigned)w_r[q] << 16), __uint_as_float((unsigned)w_i[q] << 16)});
    dft16<1>(x);
#pragma unroll
    for (int i = 1; i < 16; ++i) x[i] = cmul(x[i], tw[i]);
#pragma unroll
    for (int i = 0; i < 16; ++i) L[(i * 17 + a) * 16 + c] = x[i];
    LBAR();
#pragma unroll
    for (int p = 0; p < 16; ++p) x[p] = L[(a * 17 + p) * 16 + c];
    dft16<1>(x);
    if (ok) { cf* op = T2 + ((size_t)j * 256 + a) * KP + k;
#pragma unroll
        for (int b = 0; b < 16; ++b) op[(size_t)16 * b * KP] = x[b]; }
}
DI void b3(const cf* __restrict__ T2, u16* __restrict__ out, LAS cf* L, int s0, int tid) {
    if (tid < 289) {
        const int k = tid; cf z[8];
        const cf* ip = T2 + (size_t)s0 * KP + k;
#pragma unroll
        for (int j = 0; j < 8; ++j) z[j] = ip[(size_t)j * 256 * KP];
#pragma unroll
        for (int j = 1; j < 8; ++j) z[j] = cmul(z[j], twid((float)(s0 * j) * (1.0f / 2048.0f)));
        dft8<1>(z);
        const bool edge = (unsigned)(k - 1) >= 287u;
        const int pa = pos_in(k), pb = pos_in(576 - k);
#pragma unroll
        for (int c = 0; c < 4; ++c) {
            cf za = z[2 * c], zb = z[2 * c + 1];
            za.y = edge ? 0.f : za.y; zb.y = edge ? 0.f : zb.y;
            L[c * SEQP + pa] = (cf){za.x - zb.y, za.y + zb.x};
            if (!edge) L[c * SEQP + pb] = (cf){za.x + zb.y, zb.x - za.y};
        }
    }
    LBAR();
    fft576x4<1>(L, tid);
    if (tid < 288) {
        constexpr float SC = 1.0f / (2048.0f * 576.0f);
        const int p0 = pos_out(2 * tid), p1 = pos_out(2 * tid + 1);
#pragma unroll
        for (int c = 0; c < 4; ++c) {
            const cf a = L[c * SEQP + p0] * SC, b = L[c * SEQP + p1] * SC;
            *(unsigned*)(out + (size_t)(s0 + 256 * (2 * c)) * (2 * DM) + DM + 2 * tid) = cvtpk(a.x, b.x);
            *(unsigned*)(out + (size_t)(s0 + 256 * (2 * c + 1)) * (2 * DM) + DM + 2 * tid) = cvtpk(a.y, b.y);
        }
    }
}
}
__global__ __launch_bounds__(512) void kb1(const u16* x, fb::cf* T1) { extern __shared__ __attribute__((aligned(16))) unsigned char smem[]; fb::b1(x, T1, (LAS fb::cf*)smem, blockIdx.x, threadIdx.x); }
__global__ __launch_bounds__(256) void kb2(const fb::cf* T1, fb::cf* T2, const u16* wr, const u16* wi) { extern __shared__ __attribute__((aligned(16))) unsigned char smem[]; fb::b2(T1, T2, wr, wi, (LAS fb::cf*)smem, blockIdx.x & 7, blockIdx.x >> 3, threadIdx.x); }
__global__ __launch_bounds__(512) void kb3(const fb::cf* T2, u16* out) { extern __shared__ __attribute__((aligned(16))) unsigned char smem[]; fb::b3(T2, out, (LAS fb::cf*)smem, blockIdx.x, threadIdx.x); }
extern "C" void kernel_launch(void* const* d_in,const int* in_sizes,int n_in,void* d_out,int out_size,void* d_ws,size_t ws_size,hipStream_t stream){
  const u16* x=(const u16*)d_in[0],*c=(const u16*)d_in[1],*wq=(const u16*)d_in[2],*wuk=(const u16*)d_in[3],*wuv=(const u16*)d_in[4],*wo=(const u16*)d_in[5];
  const u16* bx=(const u16*)d_in[6],*wr=(const u16*)d_in[7],*wi=(const u16*)d_in[8];
  char* ws=(char*)d_ws; size_t T=(size_t)2048*4096*2;
  u16 *Qp=(u16*)ws,*Kp=(u16*)(ws+T),*Vt=(u16*)(ws+2*T),*ctx=(u16*)(ws+3*T);
  float* slab=(float*)(ws+4*T); unsigned* cnt=(unsigned*)(ws+4*T+(32<<20));
  fb::cf* T1=(fb::cf*)(ws+4*T+(33<<20)); fb::cf* T2=(fb::cf*)(ws+4*T+(39<<20));
  (void)hipFuncSetAttribute((const void*)k1_qkv, hipFuncAttributeMaxDynamicSharedMemorySize, 163840);
  (void)hipFuncSetAttribute((const void*)k2_attn, hipFuncAttributeMaxDynamicSharedMemorySize, 163840);
  (void)hipFuncSetAttribute((const void*)k3_oproj, hipFuncAttributeMaxDynamicSharedMemorySize, 163840);
  (void)hipMemsetAsync(cnt, 0, 256*64*4, stream);
  k1_qkv<<<256,512,163840,stream>>>(x,c,wq,wuk,wuv,Qp,Kp,Vt);
  k2_attn<<<256,512,163840,stream>>>(Qp,Kp,Vt,ctx,(unsigned long long*)(ws+(120<<20)));
  k3_oproj<<<256,512,163840,stream>>>(ctx,wo,(u16*)d_out,slab,cnt);
  kb1<<<256,512,4*648*8,stream>>>(bx,T1);
  kb2<<<152,256,16*17*16*8,stream>>>(T1,T2,wr,wi);
  kb3<<<256,512,4*648*8,stream>>>(T2,(u16*)d_out);
}
```

```cpp
#include <hip/hip_runtime.h>
#include <stdint.h>
#include <utility>
typedef unsigned short u16;
typedef short bf16x8 __attribute__((ext_vector_type(8)));
typedef short s16x4 __attribute__((ext_vector_type(4)));
typedef float f32x2 __attribute__((ext_vector_type(2)));
typedef float f32x4 __attribute__((ext_vector_type(4)));
typedef float f32x16 __attribute__((ext_vector_type(16)));
typedef unsigned u32x4 __attribute__((ext_vector_type(4)));
typedef unsigned u32x2 __attribute__((ext_vector_type(2)));
typedef __bf16 bf16x2_t __attribute__((ext_vector_type(2)));
#define LAS __attribute__((address_space(3)))
#define DI __device__ __forceinline__
#define BAR() __builtin_amdgcn_s_barrier()
#define SCHED() __builtin_amdgcn_sched_barrier(0)
#define WAIT_L0() asm volatile("s_waitcnt lgkmcnt(0)" ::: "memory")
template <int N> DI void wait_v() { asm volatile("s_waitcnt vmcnt(%0)" ::"n"(N) : "memory"); }
DI unsigned cvtpk(float lo, float hi) { f32x2 v = {lo, hi}; bf16x2_t b = __builtin_convertvector(v, bf16x2_t); return __builtin_bit_cast(unsigned, b); }
DI bf16x8 lds_b128(const LAS unsigned char* p) { return *(const LAS bf16x8*)p; }
template <int OFF> DI s16x4 lds_tr(unsigned a) { s16x4 r; asm volatile("ds_read_b64_tr_b16 %0, %1 offset:%2" : "=v"(r) : "v"(a), "n"(OFF) : "memory"); return r; }
typedef __amdgpu_buffer_rsrc_t rsrc_t;
DI rsrc_t mk_rsrc(const void* p, unsigned bytes) { return __builtin_amdgcn_make_buffer_rsrc((void*)p, 0, bytes, 0x00020000); }
DI void blds16(rsrc_t r, LAS unsigned char* l, unsigned voff, unsigned soff) { __builtin_amdgcn_raw_ptr_buffer_load_lds(r, (LAS void*)l, 16, voff, soff, 0, 0); }
DI void blds16c(rsrc_t r, LAS unsigned char* l, unsigned voff, unsigned soff) { __builtin_amdgcn_raw_ptr_buffer_load_lds(r, (LAS void*)l, 16, voff, soff, 0, 16); }
DI void bstore128c(u32x4 v, rsrc_t r, unsigned voff, unsigned soff) { __builtin_amdgcn_raw_buffer_store_b128(v, r, voff, soff, 16); SCHED(); asm volatile("s_nop 1" ::: "memory"); SCHED(); }
typedef unsigned long long u64;
typedef __attribute__((address_space(1))) u64 gu64;
DI void flag_set(u64* p, u64 tag) { __hip_atomic_store((gu64*)p, tag, __ATOMIC_RELAXED, __HIP_MEMORY_SCOPE_AGENT); }
DI u64 flag_get(const u64* p) { return __hip_atomic_load((gu64*)p, __ATOMIC_RELAXED, __HIP_MEMORY_SCOPE_AGENT); }
DI void flag_wait(const u64* p, u64 tag, bool on) {
    for (unsigned spins = 0; spins < (1u << 20); ++spins) {
        const bool ok = !on || flag_get(p) == tag;
        if (__all(ok)) break;
        __builtin_amdgcn_s_sleep(1);
    }
    asm volatile("" ::: "memory");
}
template <int N> DI void flag_waitN(const u64* p, u64 tag, int lane, int count) {
    for (unsigned spins = 0; spins < (1u << 20); ++spins) {
        u64 v[N]; bool ok = true;
#pragma unroll
        for (int i = 0; i < N; ++i) { const int ix = lane + 64 * i; v[i] = flag_get(p + (ix < count ? ix : 0)); }
#pragma unroll
        for (int i = 0; i < N; ++i) ok &= v[i] == tag;
        if (__all(ok)) break;
        __builtin_amdgcn_s_sleep(1);
    }
    asm volatile("" ::: "memory");
}
DI unsigned opaque(unsigned x) { asm volatile("" : "+v"(x)); return x; }

#define STAMPV(tsv, n) do {} while (0)
#define STAMPR(tsv, n) do {} while (0)
#define STAMPOUT(base, tsv) do {} while (0)
constexpr int SEQ = 2048, NH = 32, HD = 128, DM = 576, DC = 512, NQKV = 4096;
constexpr float QSCALE = 0.08838834764831845f * 1.4426950408889634f;

constexpr size_t TSZ = (size_t)SEQ * NQKV * 2;
constexpr size_t WS_FLAG = 0, WS_K = 1 << 20, WS_V = WS_K + TSZ, WS_CTX = WS_V + TSZ, WS_SLAB = WS_CTX + TSZ, WS_T1 = WS_SLAB + (26u << 20), WS_T2 = WS_T1 + (5u << 20), WS_STAMP = (size_t)120 << 20;
constexpr int F_EPOCH = 0, F_B1 = 64, F_KV = F_B1 + 256, F_CTX = F_KV + 32 * 64, F_B2 = F_CTX + 16 * 32 * 4, F_SLAB = F_B2 + 640;

namespace fb {
typedef f32x2 cf;
constexpr int KP = 304;
constexpr unsigned TBYTES = 8u * 256u * KP * 8u;
DI cf cmul(cf a, cf b) { return (cf){a.x * b.x - a.y * b.y, a.x * b.y + a.y * b.x}; }
DI cf cmulc(cf a, cf b) { return (cf){a.x * b.x + a.y * b.y, a.y * b.x - a.x * b.y}; }
DI cf conj(cf a) { return (cf){a.x, -a.y}; }
template <int S> DI cf muli(cf a) { return S > 0 ? (cf){-a.y, a.x} : (cf){a.y, -a.x}; }
DI cf twid(float rev) { return (cf){__builtin_amdgcn_cosf(rev), __builtin_amdgcn_sinf(rev)}; }
DI cf ldc(rsrc_t r, unsigned voff, unsigned soff) { return __builtin_bit_cast(cf, __builtin_amdgcn_raw_buffer_load_b64(r, voff, soff, 16)); }
DI void stc(rsrc_t r, unsigned voff, unsigned soff, cf v) { __builtin_amdgcn_raw_buffer_store_b64(__builtin_bit_cast(u32x2, v), r, voff, soff, 16); }
template <int S> DI void dft4(cf& a, cf& b, cf& c, cf& d) {
    const cf s0 = a + c, d0 = a - c, s1 = b + d, d1 = muli<S>(b - d);
    a = s0 + s1; c = s0 - s1; b = d0 + d1; d = d0 - d1;
}
template <int S> DI void dft8(cf (&x)[8]) {
    constexpr float R = 0.70710678118654752f;
    dft4<S>(x[0], x[2], x[4], x[6]); dft4<S>(x[1], x[3], x[5], x[7]);
    const cf o1 = (x[3] + muli<S>(x[3])) * R, o2 = muli<S>(x[5]), o3 = (muli<S>(x[7]) - x[7]) * R, o0 = x[1];
    const cf e0 = x[0], e1 = x[2], e2 = x[4], e3 = x[6];
    x[0] = e0 + o0; x[4] = e0 - o0; x[1] = e1 + o1; x[5] = e1 - o1; x[2] = e2 + o2; x[6] = e2 - o2; x[3] = e3 + o3; x[7] = e3 - o3;
}
template <int S> DI void dft3(cf& a, cf& b, cf& c) {
    constexpr float H = 0.86602540378443865f;
    const cf s = b + c, d = muli<S>(b - c) * H, m = a - s * 0.5f;
    a = a + s; b = m + d; c = m - d;
}
template <int S> DI void dft9(cf (&x)[9]) {
    constexpr float C1 = 0.76604444311897801f, S1 = 0.64278760968653933f, C2 = 0.17364817766693035f, S2 = 0.98480775301220806f, C4 = -0.93969262078590838f, S4 = 0.34202014332566873f;
    dft3<S>(x[0], x[3], x[6]); dft3<S>(x[1], x[4], x[7]); dft3<S>(x[2], x[5], x[8]);
    const cf w1 = {C1, S * S1}, w2 = {C2, S * S2}, w4 = {C4, S * S4};
    x[4] = cmul(x[4], w1); x[5] = cmul(x[5], w2); x[7] = cmul(x[7], w2); x[8] = cmul(x[8], w4);
    dft3<S>(x[0], x[1], x[2]); dft3<S>(x[3], x[4], x[5]); dft3<S>(x[6], x[7], x[8]);
    cf t;
    t = x[1]; x[1] = x[3]; x[3] = t; t = x[2]; x[2] = x[6]; x[6] = t; t = x[5]; x[5] = x[7]; x[7] = t;
}
template <int S> DI void dft16(cf (&x)[16]) {
    constexpr float C1 = 0.92387953251128674f, S1 = 0.38268343236508977f, R = 0.70710678118654752f;
#pragma unroll
    for (int n2 = 0; n2 < 4; ++n2) dft4<S>(x[n2], x[4 + n2], x[8 + n2], x[12 + n2]);
    const cf w1 = {C1, S * S1}, w2 = {R, S * R}, w3 = {S1, S * C1}, w6 = {-R, S * R}, w9 = {-C1, -S * S1};
    x[5] = cmul(x[5], w1); x[6] = cmul(x[6], w2); x[7] = cmul(x[7], w3);
    x[9] = cmul(x[9], w2); x[10] = muli<S>(x[10]); x[11] = cmul(x[11], w6);
    x[13] = cmul(x[13], w3); x[14] = cmul(x[14], w6); x[15] = cmul(x[15], w9);
#pragma unroll
    for (int k1 = 0; k1 < 4; ++k1) dft4<S>(x[4 * k1], x[4 * k1 + 1], x[4 * k1 + 2], x[4 * k1 + 3]);
#pragma unroll
    for (int a = 0; a < 4; ++a)
#pragma unroll
        for (int b = a + 1; b < 4; ++b) { const cf t = x[4 * a + b]; x[4 * a + b] = x[4 * b + a]; x[4 * b + a] = t; }
}
constexpr int SEQP = 648;
DI float bfl(unsigned w) { return __uint_as_float(w << 16); }
DI float bfh(unsigned w) { return __uint_as_float(w & 0xffff0000u); }
#define LBAR() do { WAIT_L0(); BAR(); asm volatile("" ::: "memory"); } while (0)
template <int S> DI void fft576x4(LAS cf* L, int tid) {
    if (tid < 256) {
        const int sq = tid >> 6, bc = tid & 63; LAS cf* p = L + sq * SEQP + bc; cf x[9];
#pragma unroll
        for (int a = 0; a < 9; ++a) x[a] = p[72 * a];
        dft9<S>(x);
#pragma unroll
        for (int k = 1; k < 9; ++k) x[k] = cmul(x[k], twid((float)(S * bc * k) * (1.0f / 576.0f)));
#pragma unroll
        for (int a = 0; a < 9; ++a) p[72 * a] = x[a];
    }
    LBAR();
    if (tid < 288) {
        const int sq = tid / 72, r = tid - sq * 72, k1 = r >> 3, c = r & 7; LAS cf* p = L + sq * SEQP + 72 * k1 + c; cf x[8];
#pragma unroll
        for (int b = 0; b < 8; ++b) x[b] = p[8 * b];
        dft8<S>(x);
#pragma unroll
        for (int k = 1; k < 8; ++k) x[k] = cmul(x[k], twid((float)(S * c * k) * (1.0f / 64.0f)));
#pragma unroll
        for (int b = 0; b < 8; ++b) p[8 * b] = x[b];
    }
    LBAR();
    if (tid < 288) {
        const int sq = tid / 72, r = tid - sq * 72, k1 = r >> 3, k2 = r & 7; LAS cf* p = L + sq * SEQP + 72 * k1 + 8 * k2; cf x[8];
#pragma unroll
        for (int c = 0; c < 8; ++c) x[c] = p[c];
        dft8<S>(x);
#pragma unroll
        for (int c = 0; c < 8; ++c) p[c] = x[c];
    }
    LBAR();
}
DI int pos_in(int d) { return 72 * (d >> 6) + (d & 63); }
DI int pos_out(int k) { const int k3 = k / 72, r = k - 72 * k3, k2 = r / 9, k1 = r - 9 * k2; return 72 * k1 + 8 * k2 + k3; }

DI void b1(const unsigned (&w)[8], rsrc_t rT1, LAS cf* L, int s0, int tid) {
    if (tid < 288) {
        cf v[8];
#pragma unroll
        for (int m = 0; m < 8; ++m) v[m] = (cf){bfl(w[m]), bfh(w[m])};
        constexpr float R = 0.70710678118654752f;
        const cf a0 = v[0] + v[4], a1 = v[0] - v[4], a2 = v[2] + v[6], a3 = v[2] - v[6], b0 = v[1] + v[5], b1_ = v[1] - v[5], b2 = v[3] + v[7], b3 = v[3] - v[7];
        const cf g0 = (a0 + a2) + (b0 + b2), g4 = (a0 + a2) - (b0 + b2);
        const cf g2r = a0 - a2, g2i = b2 - b0;
        const cf p = (b1_ - b3) * R, q = (b1_ + b3) * R;
        const cf g1r = a1 + p, g1i = -(a3 + q), g3r = a1 - p, g3i = a3 - q;
        const int p0 = pos_in(2 * tid);
        *(LAS f32x4*)(L + 0 * SEQP + p0) = (f32x4){g0.x, g4.x, g0.y, g4.y};
        *(LAS f32x4*)(L + 1 * SEQP + p0) = (f32x4){g1r.x, g1i.x, g1r.y, g1i.y};
        *(LAS f32x4*)(L + 2 * SEQP + p0) = (f32x4){g2r.x, g2i.x, g2r.y, g2i.y};
        *(LAS f32x4*)(L + 3 * SEQP + p0) = (f32x4){g3r.x, g3i.x, g3r.y, g3i.y};
    }
    LBAR();
    fft576x4<-1>(L, tid);
    if (tid < 289) {
        const int k = tid, pa = pos_out(k), pb = pos_out(k == 0 ? 0 : 576 - k);
        cf t[8];
        { const cf a = L[pa], b = conj(L[pb]); t[0] = (a + b) * 0.5f; t[4] = muli<-1>(a - b) * 0.5f; }
#pragma unroll
        for (int j = 1; j < 4; ++j) { t[j] = L[j * SEQP + pa]; t[8 - j] = conj(L[j * SEQP + pb]); }
        const unsigned so = (unsigned)(s0 * KP * 8);
        stc(rT1, k * 8, so, t[0]);
#pragma unroll
        for (int j = 1; j < 8; ++j) stc(rT1, k * 8, so + j * (256 * KP * 8), cmul(t[j], twid((float)(-s0 * j) * (1.0f / 2048.0f))));
    }
    LBAR();
}
DI void b3_load(cf (&z)[8], rsrc_t rT2, int s0, int tid) {
    const int k = tid < 289 ? tid : 288;
#pragma unroll
    for (int j = 0; j < 8; ++j) z[j] = ldc(rT2, k * 8, (unsigned)(s0 * KP * 8 + j * (256 * KP * 8)));
}
DI void b3(cf (&z)[8], u16* __restrict__ out, LAS cf* L, int s0, int tid) {
    if (tid < 289) {
        const int k = tid;
#pragma unroll
        for (int j = 1; j < 8; ++j) z[j] = cmul(z[j], twid((float)(s0 * j) * (1.0f / 2048.0f)));
        dft8<1>(z);
        const bool edge = (unsigned)(k - 1) >= 287u;
        const int pa = pos_in(k), pb = pos_in(576 - k);
#pragma unroll
        for (int c = 0; c < 4; ++c) {
            cf za = z[2 * c], zb = z[2 * c + 1];
            za.y = edge ? 0.f : za.y; zb.y = edge ? 0.f : zb.y;
            L[c * SEQP + pa] = (cf){za.x - zb.y, za.y + zb.x};
            if (!edge) L[c * SEQP + pb] = (cf){za.x + zb.y, zb.x - za.y};
        }
    }
    LBAR();
    fft576x4<1>(L, tid);
    if (tid < 288) {
        constexpr float SC = 1.0f / (2048.0f * 576.0f);
        const int p0 = pos_out(2 * tid), p1 = pos_out(2 * tid + 1);
#pragma unroll
        for (int c = 0; c < 4; ++c) {
            const cf a = L[c * SEQP + p0] * SC, b = L[c * SEQP + p1] * SC;
            *(unsigned*)(out + (size_t)(s0 + 256 * (2 * c)) * (2 * DM) + DM + 2 * tid) = cvtpk(a.x, b.x);
            *(unsigned*)(out + (size_t)(s0 + 256 * (2 * c + 1)) * (2 * DM) + DM + 2 * tid) = cvtpk(a.y, b.y);
        }
    }
}
}
namespace k1 {
constexpr int NS = 10, SLOT = 16384, NL = 59, NP = 50, NPRO = 8;
struct HT { int kind, kt, rd; };
constexpr HT ht(int i) {
    if (i < 32) { const int t = i >> 2, e = i & 3;
        return e == 0 ? HT{0, t, 4 * t + 1} : e == 1 ? HT{3, t, 4 * t + 1} : e == 2 ? HT{1, t, 4 * t + 2} : HT{4, t, 4 * t + 3}; }
    const int u = (i - 32) / 3, e = (i - 32) % 3;
    return e == 0 ? HT{2, u, 33 + 2 * u} : e == 1 ? HT{5, u, 33 + 2 * u} : HT{6, u, 34 + 2 * u};
}
constexpr int PB1 = 10, PKV = 43;
constexpr int nstores(int p) { return ((p >= 30 && p <= 37) ? 2 : 0) + (p == PB1 ? 1 : 0) + (p == PKV ? 1 : 0); }
struct Sched { int issue[NL]; int wait[NP + 1]; int first[NP + 2]; int kvcov; };
constexpr Sched make_sched() {
    Sched s{}; int cnt[NP + 2] = {};
    for (int i = 0; i < NL; ++i) {
        int e = i < NPRO ? 0 : 1;
        if (i >= NS && ht(i - NS).rd + 2 > e) e = ht(i - NS).rd + 2;
        if (i > 0 && s.issue[i - 1] > e) e = s.issue[i - 1];
        while (e > 0 && cnt[e] >= 2) ++e;
        s.issue[i] = e; cnt[e]++;
    }
    int endpos[NL] = {}; int tot = 0, li = 0, prev_last = -1, kvend = 1 << 30; s.kvcov = 1 << 30;
    for (int p = 0; p <= NP; ++p) {
        s.first[p] = li;
        tot += nstores(p);
        if (p == 37) kvend = tot;
        while (li < NL && s.issue[li] == p) { tot += 2; endpos[li] = tot; ++li; }
        int last = -1; for (int i = 0; i < NL; ++i) if (ht(i).rd <= p + 1) last = i;
        if (last > prev_last) { s.wait[p] = (s.issue[last] <= p) ? tot - endpos[last] : -1000; prev_last = last;
            if (s.wait[p] >= 0 && tot - s.wait[p] >= kvend && s.kvcov > p) s.kvcov = p; } else s.wait[p] = -1;
    }
    s.first[NP + 1] = li;
    return s;
}
constexpr Sched SC = make_sched();
constexpr bool sched_ok() { for (int p = 0; p <= NP; ++p) if (SC.wait[p] < -1 || SC.wait[p] > 63) return false; return SC.first[NP + 1] == NL; }
static_assert(sched_ok(), "schedule");
static_assert(SC.kvcov < PKV, "the KV stores must be covered by a wait before the flag");

struct St {
    LAS unsigned char* lds; unsigned tsv;
    rsrc_t rw[3], ra[2]; unsigned sw, sa[3];
    u64 tag; u64 *fb1, *fkv;
    unsigned voffW[2], voffC[2], voffX[2];
    unsigned ldsw, aoff, boff[2];
    f32x4 acc[2][2][4][2];
    bf16x8 At[4][2]; s16x4 B0[2][2][2], B1[2][2][2];
    u32x4 pk[16]; rsrc_t rK, rV; unsigned evo, skq, sv;
};
template <int I> DI void stage(St& s) {
    constexpr HT h = ht(I); constexpr int slot = I % NS;
    LAS unsigned char* dst = s.lds + slot * SLOT + s.ldsw;
    if constexpr (h.kind <= 2) { const unsigned so = s.sw + h.kt * (64 * NQKV * 2); blds16(s.rw[h.kind], dst, s.voffW[0], so); blds16(s.rw[h.kind], dst + 8192, s.voffW[1], so); }
    else if constexpr (h.kind <= 4) { const unsigned so = s.sa[0] + (h.kind - 3) * (128 * DC * 2) + h.kt * 128; blds16(s.ra[0], dst, s.voffC[0], so); blds16(s.ra[0], dst + 8192, s.voffC[1], so); }
    else { const unsigned so = s.sa[h.kind - 4] + h.kt * 128; blds16(s.ra[1], dst, s.voffX[0], so); blds16(s.ra[1], dst + 8192, s.voffX[1], so); }
}
template <int I> DI void lda(St& s) {
    constexpr int slot = I % NS;
    const unsigned a0 = opaque(s.aoff + slot * SLOT), a1 = opaque((s.aoff ^ 64) + slot * SLOT);
#pragma unroll
    for (int m = 0; m < 4; ++m) { s.At[m][0] = lds_b128((const LAS unsigned char*)a0 + m * 2048); s.At[m][1] = lds_b128((const LAS unsigned char*)a1 + m * 2048); }
}
template <int I> DI void ldb(St& s, s16x4 (&B)[2][2][2]) {
    constexpr int slot = I % NS;
#pragma unroll
    for (int n = 0; n < 2; ++n) {
        const unsigned p = s.boff[n] + slot * SLOT;
        B[n][0][0] = lds_tr<0>(p); B[n][0][1] = lds_tr<1024>(p); B[n][1][0] = lds_tr<8192>(p); B[n][1][1] = lds_tr<9216>(p);
    }
}
template <bool TR, bool ZERO> DI void mma(f32x4 (&acc)[4][2], const bf16x8 (&At)[4][2], const s16x4 (&Bh)[2][2][2]) {
    bf16x8 B[2][2];
#pragma unroll
    for (int n = 0; n < 2; ++n)
#pragma unroll
        for (int k = 0; k < 2; ++k) B[n][k] = __builtin_shufflevector(Bh[n][k][0], Bh[n][k][1], 0, 1, 2, 3, 4, 5, 6, 7);
    __builtin_amdgcn_s_setprio(1);
#pragma unroll
    for (int k = 0; k < 2; ++k)
#pragma unroll
        for (int m = 0; m < 4; ++m)
#pragma unroll
            for (int n = 0; n < 2; ++n) {
                const f32x4 c = (ZERO && k == 0) ? (f32x4){0.f, 0.f, 0.f, 0.f} : acc[m][n];
                acc[m][n] = TR ? __builtin_amdgcn_mfma_f32_16x16x32_bf16(B[n][k], At[m][k], c, 0, 0, 0)
                               : __builtin_amdgcn_mfma_f32_16x16x32_bf16(At[m][k], B[n][k], c, 0, 0, 0);
            }
    __builtin_amdgcn_s_setprio(0);
}
template <bool SCALE> DI void pack_t(const f32x4 (&a)[4][2], u32x4* w) {
#pragma unroll
    for (int m = 0; m < 4; ++m) {
        f32x4 v0 = a[m][0], v1 = a[m][1];
        if (SCALE) { v0 *= QSCALE; v1 *= QSCALE; }
        w[m] = (u32x4){cvtpk(v0[0], v0[1]), cvtpk(v0[2], v0[3]), cvtpk(v1[0], v1[1]), cvtpk(v1[2], v1[3])};
        asm volatile("" : "+v"(w[m]));
    }
}
DI void pack_v(const f32x4 (&a)[4][2], u32x4* w) {
#pragma unroll
    for (int u = 0; u < 2; ++u)
#pragma unroll
        for (int n = 0; n < 2; ++n) {
            const f32x4 v0 = a[2 * u][n], v1 = a[2 * u + 1][n];
            w[2 * u + n] = (u32x4){cvtpk(v0[0], v0[1]), cvtpk(v0[2], v0[3]), cvtpk(v1[0], v1[1]), cvtpk(v1[2], v1[3])};
            asm volatile("" : "+v"(w[2 * u + n]));
        }
}
template <int I> DI void store1(St& s) {
    constexpr int qd = I >> 2, i = I & 3, ai = qd >> 1;
    if constexpr ((qd & 1) == 0) bstore128c(s.pk[I], s.rK, s.evo, s.skq + (ai * 8 + i) * 4096);
    else bstore128c(s.pk[I], s.rV, s.evo, s.sv + (i & 1) * 65536 + (ai * 4 + (i >> 1)) * 1024);
}
DI void mk_voffx(St& s) {
    const int tid = (int)opaque(threadIdx.x);
#pragma unroll
    for (int i = 0; i < 2; ++i) { const int b = tid * 16 + i * 8192, r = b >> 7, kc = ((b >> 4) & 7) ^ ((r >> 1) & 7), pr = (r & ~24) | ((r & 8) << 1) | ((r & 16) >> 1);
        s.voffX[i] = (unsigned)(pr * DM * 2 + kc * 16); }
}
template <int A, int B> DI void stage_range(St& s) { if constexpr (A < B) { stage<A>(s); stage_range<A + 1, B>(s); } }
template <int P> DI void phase(St& s) {
    if constexpr (P <= 32) {
        constexpr int t = (P - 1) >> 2, e = (P - 1) & 3;
        if constexpr (e == 0) { ldb<4 * t>(s, s.B0); lda<4 * t + 1>(s); }
        if constexpr (e == 1) ldb<4 * t + 2>(s, s.B1);
        if constexpr (e == 2) lda<4 * t + 3>(s);
    } else {
        constexpr int u = (P - 33) >> 1, e = (P - 33) & 1;
        if constexpr (e == 0) { ldb<32 + 3 * u>(s, s.B0); lda<33 + 3 * u>(s); }
        else lda<34 + 3 * u>(s);
    }
    if constexpr (P == 30) { const int l_ = (int)opaque(threadIdx.x) & 63; s.evo = (unsigned)((l_ & 15) * 64 + (l_ >> 4) * 16); pack_t<false>(s.acc[0][0], s.pk); }
    if constexpr (P == 31) pack_v(s.acc[0][1], s.pk + 4);
    if constexpr (P == 32) pack_t<false>(s.acc[1][0], s.pk + 8);
    if constexpr (P == 33) pack_v(s.acc[1][1], s.pk + 12);
    if constexpr (P >= 30 && P <= 37) { store1<2 * (P - 30)>(s); store1<2 * (P - 30) + 1>(s); }
    if constexpr (P == PB1) { if ((threadIdx.x & 63) == 0) flag_set(s.fb1, s.tag); }
    if constexpr (P == PKV) { if ((threadIdx.x & 63) == 0) flag_set(s.fkv, s.tag); }
    if constexpr (P == SC.issue[33]) mk_voffx(s);
    stage_range<SC.first[P], SC.first[P + 1]>(s);
    if constexpr (SC.wait[P] >= 0) wait_v<SC.wait[P]>();
    BAR(); WAIT_L0(); SCHED();
    if constexpr (P <= 32) {
        constexpr int t = (P - 1) >> 2, e = (P - 1) & 3;
        if constexpr (e == 0) mma<true, t == 0>(s.acc[0][0], s.At, s.B0);
        if constexpr (e == 1) mma<false, t == 0>(s.acc[0][1], s.At, s.B1);
        if constexpr (e == 2) mma<true, t == 0>(s.acc[1][0], s.At, s.B0);
        if constexpr (e == 3) mma<false, t == 0>(s.acc[1][1], s.At, s.B1);
    } else {
        constexpr int u = (P - 33) >> 1, e = (P - 33) & 1;
        mma<true, u == 0>(s.acc[e][0], s.At, s.B0);
    }
    BAR(); SCHED();
    if constexpr (P % 5 == 0) STAMPV(s.tsv, 2 + P / 5);
}
template <int... Ps> DI void phases(St& s, std::integer_sequence<int, Ps...>) { (phase<Ps + 1>(s), ...); }
}

namespace k2 {
constexpr int UNIT = 65536, VOFF = 32768, XTRA = 131072;
constexpr float THR = 8.0f;
#define K2_PD 4
constexpr int PD = K2_PD;
#define MFMA32(a, b, c) __builtin_amdgcn_mfma_f32_32x32x16_bf16((a), (b), (c), 0, 0, 0)
#define PIN(x) asm volatile("" : "+v"(x))
DI float max2f(float a, float b) { return __builtin_elementwise_maximum(a, b); }
DI float max3f(float a, float b, float c) { return max2f(max2f(a, b), c); }
DI float swap_max(float x) { auto r = __builtin_amdgcn_permlane32_swap(__float_as_uint(x), __float_as_uint(x), false, false); return max2f(__uint_as_float(r[0]), __uint_as_float(r[1])); }
DI float swap_add(float x) { auto r = __builtin_amdgcn_permlane32_swap(__float_as_uint(x), __float_as_uint(x), false, false); return __uint_as_float(r[0]) + __uint_as_float(r[1]); }
DI void mask16(f32x16& s, int dq) {
    const float NEG = -__builtin_inff();
#pragma unroll
    for (int r = 0; r < 16; ++r) { const int c = (r & 3) + 8 * (r >> 2); s[r] = c > dq ? NEG : s[r]; }
}
struct St {
    unsigned ka[8];
    bf16x8 qr[8], fr[16 + 8]; u32x4 p[2][2];
    f32x16 o[4], s[2], negm;
    float l0, l1, e[16], mxa, mxb; int dq;
};
template <int I, int POS, bool NOPV> DI void ldfrag(St& c) {
    if constexpr (I < 8) c.fr[I] = lds_b128((const LAS unsigned char*)c.ka[I] + POS * 8192);
    else if constexpr (I < 16) { if constexpr (!NOPV) c.fr[I] = lds_b128((const LAS unsigned char*)c.ka[2 * POS + ((I - 8) >> 2)] + VOFF + ((I - 8) & 3) * 8192); }
    else if constexpr (POS < 3) c.fr[I] = lds_b128((const LAS unsigned char*)c.ka[I - 16] + (POS + 1) * 8192);
}
template <int X, int E> DI void do_exp(St& c) { c.e[E] = __builtin_amdgcn_exp2f(c.s[X][E]); PIN(c.e[E]); }
template <int X, int E> DI void do_fin(St& c) {
    if constexpr (E & 1) { c.l1 += c.e[E]; PIN(c.l1); unsigned w = cvtpk(c.e[E - 1], c.e[E]); PIN(w); c.p[X][E >> 3][(E >> 1) & 3] = w; }
    else { c.l0 += c.e[E]; PIN(c.l0); }
}
template <int X, int I> DI void do_max(St& c) {
    const f32x16& v = c.s[X];
    if constexpr (I == 0) c.mxa = max2f(v[0], v[1]);
    else if constexpr (I == 1) c.mxb = max2f(v[2], v[3]);
    else if constexpr (I & 1) c.mxb = max3f(c.mxb, v[2 * I], v[2 * I + 1]);
    else c.mxa = max3f(c.mxa, v[2 * I], v[2 * I + 1]);
    if constexpr (I & 1) PIN(c.mxb); else PIN(c.mxa);
}
template <int G, int POS, bool NOPV> DI void gapA(St& c) {
    constexpr int X = POS & 1;
    c.s[X] = MFMA32(c.fr[G], c.qr[G], G == 0 ? c.negm : c.s[X]);
    ldfrag<G + PD, POS, NOPV>(c);
    if constexpr (G >= 1) do_fin<X ^ 1, G - 1>(c);
    do_exp<X ^ 1, G>(c);
    SCHED();
}
template <int G, int POS, bool NOPV> DI void gapB(St& c) {
    constexpr int X = POS & 1, ks = G >> 2, db = G & 3;
    if constexpr (!NOPV) c.o[db] = MFMA32(c.fr[8 + G], __builtin_bit_cast(bf16x8, c.p[X][ks]), c.o[db]);
    ldfrag<8 + G + PD, POS, NOPV>(c);
    if constexpr (G == 0) { do_fin<X ^ 1, 7>(c); do_exp<X ^ 1, 8>(c); do_exp<X ^ 1, 9>(c); }
    if constexpr (G == 1) { do_fin<X ^ 1, 8>(c); do_fin<X ^ 1, 9>(c); do_exp<X ^ 1, 10>(c); }
    if constexpr (G >= 2) do_fin<X ^ 1, 8 + G>(c);
    if constexpr (G >= 2 && G <= 6) do_exp<X ^ 1, 9 + G>(c);
    if constexpr (G == 2) { do_max<X, 0>(c); do_max<X, 1>(c); }
    if constexpr (G == 3) { do_max<X, 2>(c); }
    if constexpr (G == 4) { do_max<X, 3>(c); do_max<X, 4>(c); }
    if constexpr (G == 5) { do_max<X, 5>(c); }
    if constexpr (G == 6) { do_max<X, 6>(c); do_max<X, 7>(c); }
    if constexpr (G == 7) { c.mxa = max2f(c.mxa, c.mxb); PIN(c.mxa); }
    SCHED();
}
template <int POS, bool NOPV, int O, int... Gs> DI void phaseA(St& c, std::integer_sequence<int, Gs...>) { (gapA<Gs + O, POS, NOPV>(c), ...); }
template <int POS, bool NOPV, int O, int... Gs> DI void phaseB(St& c, std::integer_sequence<int, Gs...>) { (gapB<Gs + O, POS, NOPV>(c), ...); }
template <int POS, int... Is> DI void ldfirst(St& c, std::integer_sequence<int, Is...>) { (ldfrag<Is, POS, false>(c), ...); }
template <int... Is> DI void takefirst(St& c, std::integer_sequence<int, Is...>) { ((c.fr[Is] = c.fr[16 + Is]), ...); }
template <int X, int O, int... Es> DI void exps(St& c, std::integer_sequence<int, Es...>) { (do_exp<X, Es + O>(c), ...); }
template <int X, int O, int... Es> DI void fins(St& c, std::integer_sequence<int, Es...>) { (do_fin<X, Es + O>(c), ...); }
#define ISEQ(n) std::make_integer_sequence<int, n>{}
DI void rescale_o(St& c, float f) {
#pragma unroll
    for (int b = 0; b < 4; ++b)
#pragma unroll
        for (int r = 0; r < 16; ++r) c.o[b][r] *= f;
}
DI float max16(const f32x16& a) {
    float m0 = max3f(a[0], a[1], a[2]), m1 = max3f(a[3], a[4], a[5]);
    m0 = max3f(m0, a[6], a[7]); m1 = max3f(m1, a[8], a[9]); m0 = max3f(m0, a[10], a[11]); m1 = max3f(m1, a[12], a[13]);
    return max3f(m0, m1, max2f(a[14], a[15]));
}
template <int POS, bool NOPV, bool FIRST> DI void halfstep(St& c, bool diag) {
    constexpr int X = POS & 1;
    if constexpr (POS == 0) { ldfirst<0>(c, ISEQ(PD)); SCHED(); } else takefirst(c, ISEQ(PD));
    phaseA<POS, NOPV, 0>(c, ISEQ(8));
    phaseB<POS, NOPV, 0>(c, ISEQ(8));
    asm volatile("" ::: "memory");
    const float thr = diag ? -__builtin_inff() : THR;
    if (FIRST || __builtin_expect(__any(c.mxa > thr), 0)) {
        mask16(c.s[X], diag ? c.dq - 32 * POS : 64);
        const float mx = swap_max(max16(c.s[X]));
        if (FIRST || __any(mx > THR)) {
            const float dl = FIRST ? mx : max2f(mx, 0.f);
            if constexpr (!FIRST) {
                const float f = __builtin_amdgcn_exp2f(-dl);
                c.l0 *= f; c.l1 *= f; rescale_o(c, f);
#pragma unroll
                for (int i = 0; i < 8; ++i) { const unsigned w = c.p[X ^ 1][i >> 2][i & 3]; c.p[X ^ 1][i >> 2][i & 3] = cvtpk(__uint_as_float(w << 16) * f, __uint_as_float(w & 0xffff0000u) * f); }
            }
#pragma unroll
            for (int r = 0; r < 16; ++r) { c.s[X][r] -= dl; c.negm[r] -= dl; }
        }
    }
}
DI void pv_tail(St& c, const u32x4 (&p)[2], unsigned abase, int pos) {
#pragma unroll
    for (int ks = 0; ks < 2; ++ks) {
        const unsigned a = abase ^ (unsigned)((2 * pos + ks) << 5);
        bf16x8 f[4];
#pragma unroll
        for (int b = 0; b < 4; ++b) f[b] = lds_b128((const LAS unsigned char*)a + VOFF + b * 8192);
#pragma unroll
        for (int b = 0; b < 4; ++b) c.o[b] = MFMA32(f[b], __builtin_bit_cast(bf16x8, p[ks]), c.o[b]);
    }
}
}

namespace k3 {
constexpr int ABYTES = 16384, BBYTES = 18432, STG = ABYTES + BBYTES, NKT = 16, NSTG = 4;
constexpr int SLABQ = 1024;
struct St {
    LAS unsigned char* lds; unsigned tsv; rsrc_t rA, rB; unsigned voffA[2], voffB[3], sa, sb, ldsw; bool extra;
    unsigned aoff[2], boff;
    f32x4 acc[2][5];
    bf16x8 af[2][2]; s16x4 bf[2][5][2];
};
DI void stageA(St& s, int buf, int Tc) {
    LAS unsigned char* d = s.lds + buf * STG + s.ldsw;
    blds16c(s.rA, d, s.voffA[0], s.sa + Tc * 4096); blds16c(s.rA, d + 8192, s.voffA[1], s.sa + Tc * 4096);
}
DI void stageB(St& s, int buf, int Tc) {
    LAS unsigned char* d = s.lds + buf * STG + s.ldsw;
    blds16(s.rB, d + ABYTES, s.voffB[0], s.sb + Tc * (64 * DM * 2)); blds16(s.rB, d + ABYTES + 8192, s.voffB[1], s.sb + Tc * (64 * DM * 2));
    if (s.extra) blds16(s.rB, d + ABYTES + 16384, s.voffB[2], s.sb + Tc * (64 * DM * 2));
}
DI void stage(St& s, int buf, int Tc) { stageA(s, buf, Tc); stageB(s, buf, Tc); }
template <int I, int NC, int BUF, int KS, int X> DI void ldone(St& s, unsigned a, unsigned b) {
    if constexpr (I < 2) s.af[X][I] = lds_b128((const LAS unsigned char*)a + I * 2048);
    else if constexpr (I < 2 + 2 * NC) { constexpr int c = (I - 2) >> 1, hf = (I - 2) & 1; s.bf[X][c][hf] = lds_tr<c * 32 + hf * 16 * 288 + KS * 32 * 288>(b); }
}
template <int M, int NC, int BUF, int KS, int X, bool ZERO> DI void gap(St& s, unsigned a, unsigned b) {
    constexpr int c = M >> 1, m = M & 1;
    const bf16x8 bb = __builtin_shufflevector(s.bf[X][c][0], s.bf[X][c][1], 0, 1, 2, 3, 4, 5, 6, 7);
    s.acc[m][c] = __builtin_amdgcn_mfma_f32_16x16x32_bf16(bb, s.af[X][m], ZERO ? (f32x4){0.f, 0.f, 0.f, 0.f} : s.acc[m][c], 0, 0, 0);
    ldone<2 * M, NC, BUF, KS, X ^ 1>(s, a, b); ldone<2 * M + 1, NC, BUF, KS, X ^ 1>(s, a, b);
    SCHED();
}
template <int NC, int BUF, int KS, int X, bool ZERO, int... Ms> DI void kstep(St& s, std::integer_sequence<int, Ms...>) {
    const unsigned a = opaque(s.aoff[KS] + BUF * STG), b = s.boff + BUF * STG;
    (gap<Ms, NC, BUF, KS, X, ZERO>(s, a, b), ...);
    WAIT_L0(); SCHED();
}
template <int N> DI void waitv2(bool extra) { if (extra) wait_v<N * 5>(); else wait_v<N * 4>(); }
template <int NC, int B0, int W = 2, bool STG_ = true, int FIX = -1> DI void period(St& s, int T) {
    constexpr int B1 = (B0 + 1) & 3;
    if constexpr (FIX >= 0) wait_v<FIX>(); else waitv2<W>(s.extra);
    BAR(); SCHED();
    if constexpr (STG_) stage(s, B0, T + 4);
    SCHED();
    kstep<NC, B1, 0, 1, false>(s, std::make_integer_sequence<int, 2 * NC>{});
    kstep<NC, B1, 1, 0, false>(s, std::make_integer_sequence<int, 2 * NC>{});
}
template <int NC, int... Is> DI void ldall(St& s, std::integer_sequence<int, Is...>) { const unsigned a = opaque(s.aoff[0]), b = s.boff; (ldone<Is, NC, 0, 0, 0>(s, a, b), ...); }
template <int NC> DI void mainloop(St& s) {
    ldall<NC>(s, std::make_integer_sequence<int, 2 + 2 * NC>{});
    WAIT_L0(); SCHED();
    kstep<NC, 0, 1, 0, true>(s, std::make_integer_sequence<int, 2 * NC>{});
    period<NC, 0, 2, true, 4>(s, 0); period<NC, 1, 2, true, 6>(s, 1); period<NC, 2>(s, 2); period<NC, 3>(s, 3);
    for (int T = 4; T < 12; T += 4) { period<NC, 0>(s, T); period<NC, 1>(s, T + 1); period<NC, 2>(s, T + 2); period<NC, 3>(s, T + 3); }
    period<NC, 0, 2, false>(s, 12); period<NC, 1, 1, false>(s, 13); period<NC, 2, 0, false>(s, 14);
#pragma unroll
    for (int c = 0; c < NC; ++c) {
        const bf16x8 bb = __builtin_shufflevector(s.bf[1][c][0], s.bf[1][c][1], 0, 1, 2, 3, 4, 5, 6, 7);
#pragma unroll
        for (int m = 0; m < 2; ++m) s.acc[m][c] = __builtin_amdgcn_mfma_f32_16x16x32_bf16(bb, s.af[1][m], s.acc[m][c], 0, 0, 0);
    }
}
}

struct Params { const u16 *x, *c, *wq, *wuk, *wuv, *wo, *bx, *wr, *wi; u16* out; char* ws; };

__global__ __launch_bounds__(512, 2) void fused(const u16* __restrict__ x, const u16* __restrict__ cin, const u16* __restrict__ wq_, const u16* __restrict__ wuk, const u16* __restrict__ wuv,
                                                const u16* __restrict__ wo, const u16* __restrict__ bx, const u16* __restrict__ wr_, const u16* __restrict__ wi_, u16* __restrict__ out, char* __restrict__ ws) {
    extern __shared__ __attribute__((aligned(16))) unsigned char smem[];
    LAS unsigned char* lds = (LAS unsigned char*)smem;
    const unsigned lbase = (unsigned)(uintptr_t)lds;
    unsigned tsv = 0x7f7f7f7f; STAMPV(tsv, 0); STAMPR(tsv, 62);
    const int bid = blockIdx.x, xcd = bid & 7, idx = bid >> 3, h = xcd * 4 + (idx & 3), pr = idx >> 2, qL = 15 - pr;
    const int wid = __builtin_amdgcn_readfirstlane(threadIdx.x >> 6);
    u64* const flags = (u64*)(ws + WS_FLAG);
    const rsrc_t rK = mk_rsrc(ws + WS_K, (unsigned)TSZ), rV = mk_rsrc(ws + WS_V, (unsigned)TSZ);
    const rsrc_t rT1 = mk_rsrc(ws + WS_T1, fb::TBYTES), rT2 = mk_rsrc(ws + WS_T2, fb::TBYTES);
    u64 tag;
    k2::St c;
    {
        using namespace k1;
        St s; s.lds = lds; s.tsv = tsv;
        const int tid = threadIdx.x, lane = tid & 63, wr = wid >> 2, wc = wid & 3, fr = lane & 15, fq = lane >> 4;
        const u64 ep = flag_get(flags + F_EPOCH);
        unsigned bw[8];
        { const int t2 = tid < 288 ? tid : 287;
#pragma unroll
          for (int m = 0; m < 8; ++m) bw[m] = *(const unsigned*)(bx + (size_t)(bid + 256 * m) * DM + 2 * t2); }
        s.rw[0] = mk_rsrc(wuk, DC * NQKV * 2); s.rw[1] = mk_rsrc(wuv, DC * NQKV * 2); s.rw[2] = mk_rsrc(wq_, DM * NQKV * 2);
        s.ra[0] = mk_rsrc(cin, SEQ * DC * 2); s.ra[1] = mk_rsrc(x, SEQ * DM * 2);
        s.sw = h * HD * 2; s.sa[0] = pr * 256 * DC * 2; s.sa[1] = qL * 128 * DM * 2; s.sa[2] = pr * 128 * DM * 2;
        s.rK = rK; s.rV = rV;
        s.skq = (unsigned)(((h * 128 + pr * 16 + wr * 4) * 4 + wc) * 1024);
        s.sv = (unsigned)(((h * 8 + 2 * wc) * 64 + pr * 8 + wr * 2) * 1024);
        s.fb1 = flags + F_B1 + bid; s.fkv = flags + F_KV + h * 64 + pr * 8 + wid;
#pragma unroll
        for (int i = 0; i < 2; ++i) {
            const int b = tid * 16 + i * 8192;
            { const int k = b >> 8, u = (b >> 5) & 7, hf = (b >> 4) & 1, f = (k & 3) | (((k >> 3) & 1) << 2); s.voffW[i] = (unsigned)(k * NQKV * 2 + ((u ^ f) * 16 + hf * 8) * 2); }
            { const int r = b >> 7, kc = ((b >> 4) & 7) ^ ((r >> 1) & 7), prr = (r & ~24) | ((r & 8) << 1) | ((r & 16) >> 1);
              s.voffC[i] = (unsigned)(prr * DC * 2 + kc * 16); }
        }
        s.ldsw = wid * 1024;
        stage_range<0, NPRO>(s);
        SCHED();
        tag = (((u64)__builtin_amdgcn_readfirstlane((unsigned)(ep >> 32)) << 32) | (u64)__builtin_amdgcn_readfirstlane((unsigned)ep)) + 1ull; s.tag = tag;
        fb::b1(bw, rT1, (LAS fb::cf*)(lds + 8 * SLOT), bid, tid);
        s.aoff = lbase + (unsigned)((wr * 64 + fr) * 128 + ((fq ^ ((fr >> 1) & 7)) * 16));
        { const int q = (lane & 15) >> 2, p = lane & 3, f = q | ((fq & 1) << 2);
#pragma unroll
          for (int n = 0; n < 2; ++n) s.boff[n] = lbase + (unsigned)(256 * (8 * fq + q) + 32 * ((2 * wc + n) ^ f) + 8 * p); }
        wait_v<SC.wait[0]>();
        BAR();
        if (wr == 1) BAR();
        STAMPV(s.tsv, 1);
        phases(s, std::make_integer_sequence<int, NP>{});
        if (wr == 0) BAR();
        pack_t<true>(s.acc[0][0], s.pk); pack_t<true>(s.acc[1][0], s.pk + 4);
        { const int l_ = (int)opaque(threadIdx.x) & 63, fr_ = l_ & 15, fq_ = l_ >> 4;
#pragma unroll
          for (int i = 0; i < 8; ++i) { const int m = i & 3, row = wr * 64 + 32 * (m >> 1) + 16 * (fr_ >> 3) + 8 * (m & 1) + (fr_ & 7);
              *(LAS u32x4*)(lds + 98304 + (i >> 2) * 32768 + row * 256 + (((wc * 4 + fq_) ^ (row & 15)) << 4)) = s.pk[i]; } }
        tsv = s.tsv;
    }
    STAMPV(tsv, 13);
    {
        using namespace k2;
        const int tid = (int)opaque(threadIdx.x), lane = tid & 63, wq = wid & 3, grp = wid >> 2, r32 = lane & 31, hi = lane >> 5;
        const int qb = grp == 0 ? qL : pr;
        const unsigned hb = (unsigned)h * (SEQ * HD * 2);
        const int a0 = wq, srow = 4 * a0 + (lane >> 4), sch = (lane & 15) ^ srow;
        const unsigned voffK = (unsigned)((sch >> 2) * 1024 + (4 * (a0 & 1) + (lane >> 4)) * 64 + (sch & 3) * 16 + (a0 >> 1) * 4096);
        const unsigned voffV = (unsigned)((sch >> 2) * 1024 + srow * 64 + (sch & 3) * 16);
        const unsigned ldsw = (unsigned)(4 * a0 * 256);
        int nst = 16;
#define STAGE_K(t_) do { _Pragma("unroll") for (int j_ = 0; j_ < 8; ++j_) blds16c(rK, lds + ((t_) & 1) * UNIT + ldsw + j_ * 4096, voffK, hb + (unsigned)(t_) * 32768u + (j_ >> 1) * 8192 + (j_ & 1) * 512); } while (0)
#define STAGE_V(t_) do { const unsigned vo_ = ((t_) == 0 || (t_) == 16) ? (voffV & ~2048u) : voffV, so_ = hb + ((t_) == 0 ? 0u : (unsigned)(t_) * 4096u - 2048u); \
        _Pragma("unroll") for (int j_ = 0; j_ < 8; ++j_) blds16c(rV, lds + ((t_) & 1) * UNIT + VOFF + ldsw + j_ * 4096, vo_, so_ + j_ * 65536); } while (0)
#define SYNC_S(t_) do { wait_v<0>(); WAIT_L0(); BAR(); asm volatile("" ::: "memory"); if ((t_) + 2 <= qL) STAGE_K((t_) + 2); if ((t_) + 2 <= qL + 1) STAGE_V((t_) + 2); } while (0)
#define SYNC(t_) do { if (grp == 1) SYNC_S(t_); else BAR(); \
        _Pragma("unroll") for (int i_ = 0; i_ < 8; ++i_) c.ka[i_] ^= (unsigned)UNIT; STAMPV(tsv, nst); ++nst; } while (0)
        flag_wait(flags + F_KV + h * 64 + lane, tag, true);
        if (grp == 0) { STAGE_K(0); } else { STAGE_V(0); STAGE_K(1); }
#pragma unroll
        for (int d = 0; d < 8; ++d) c.ka[d] = (lbase + (unsigned)(r32 * 256 + ((hi ^ (r32 & 15)) << 4))) ^ (unsigned)(d << 5);
        WAIT_L0(); BAR(); asm volatile("" ::: "memory");
#pragma unroll
        for (int d = 0; d < 8; ++d) c.qr[d] = lds_b128((const LAS unsigned char*)(opaque(c.ka[d] + 98304 + grp * 32768 + wq * 8192)));
        c.dq = 32 * wq + r32 - 4 * hi;
#pragma unroll
        for (int r = 0; r < 16; ++r) { c.o[0][r] = 0.f; c.o[1][r] = 0.f; c.o[2][r] = 0.f; c.o[3][r] = 0.f; c.negm[r] = 0.f; c.s[1][r] = -__builtin_inff(); }
        c.l0 = 0.f; c.l1 = 0.f;
        if (grp == 0) wait_v<0>(); else wait_v<8>();
        WAIT_L0(); BAR(); asm volatile("" ::: "memory");
        if (grp == 1) STAGE_V(1);
        STAMPV(tsv, 14);
        int t = 0;
        halfstep<0, true, true>(c, qb == 0 && wq == 0);
        halfstep<1, true, false>(c, qb == 0 && wq <= 1);
        for (; t < qb;) {
            halfstep<2, false, false>(c, false);
            halfstep<3, false, false>(c, false);
            SYNC(t); ++t;
            const bool last = t == qb;
            halfstep<0, false, false>(c, last && wq == 0);
            halfstep<1, false, false>(c, last && wq <= 1);
        }
        if (wq >= 2) {
            halfstep<2, false, false>(c, wq == 2);
            halfstep<3, false, false>(c, true);
            SYNC(t); ++t;
        }
        { const int pos = wq >= 2 ? 0 : 2;
          pv_tail(c, c.p[0], c.ka[0], pos);
          exps<1, 0>(c, ISEQ(16)); fins<1, 0>(c, ISEQ(16));
          pv_tail(c, c.p[1], c.ka[0], pos + 1); }
        { const float rl = 1.0f / swap_add(c.l0 + c.l1);
          const rsrc_t rC = mk_rsrc(ws + WS_CTX, (unsigned)TSZ);
          const unsigned so = (unsigned)(((qb * 4 + wq) * 128 + h * 4) * 2048), vo = (unsigned)(r32 * 64 + 16 * hi);
#pragma unroll
          for (int b = 0; b < 4; ++b)
#pragma unroll
              for (int uu = 0; uu < 2; ++uu) {
                  float v[8];
#pragma unroll
                  for (int j = 0; j < 8; ++j) v[j] = c.o[b][8 * uu + j] * rl;
                  bstore128c((u32x4){cvtpk(v[0], v[1]), cvtpk(v[2], v[3]), cvtpk(v[4], v[5]), cvtpk(v[6], v[7])}, rC, vo, so + 2048 * b + 32 * uu);
              }
          wait_v<0>();
          if (lane == 0) flag_set(flags + F_CTX + (qb * 32 + h) * 4 + wq, tag); }
        const int task = pr * 32 + h;
        if (grp == 1 && task < 152) {
            using namespace fb;
            for (; t <= qb + 1; ++t) SYNC_S(t);
            const int j = task & 7, kg = task >> 3, tt = tid - 256, cc = tt & 15, a = tt >> 4, k = 16 * kg + cc; const bool ok = k < 289; const int kk = ok ? k : 288;
            LAS cf* L = (LAS cf*)(lds + XTRA);
            u16 w_r[16], w_i[16];
#pragma unroll
            for (int q = 0; q < 16; ++q) { const size_t o = (size_t)(8 * (a + 16 * q) + j) * 289 + kk; w_r[q] = wr_[o]; w_i[q] = wi_[o]; }
            flag_waitN<4>(flags + F_B1, tag, lane, 256);
            if (bid == 0 && tt == 0) flag_set(flags + F_EPOCH, tag);
            unsigned wpk[16];
#pragma unroll
            for (int q = 0; q < 16; ++q) { wpk[q] = (unsigned)w_r[q] | ((unsigned)w_i[q] << 16); PIN(wpk[q]); }
            SYNC_S(t); ++t;
            cf xx[16], tw[16];
            const unsigned tvo = (unsigned)((a * KP + kk) * 8), tso = (unsigned)(j * 256 * KP * 8);
#pragma unroll
            for (int b = 0; b < 16; ++b) xx[b] = ldc(rT1, tvo, tso + b * (16 * KP * 8));
#pragma unroll
            for (int p = 1; p < 16; ++p) tw[p] = twid((float)(a * p) * (1.0f / 256.0f));
            dft16<-1>(xx);
#pragma unroll
            for (int p = 1; p < 16; ++p) xx[p] = cmulc(xx[p], tw[p]);
#pragma unroll
            for (int p = 0; p < 16; ++p) L[(a * 16 + p) * 16 + cc] = xx[p];
            SYNC_S(t); ++t;
#pragma unroll
            for (int i = 0; i < 16; ++i) xx[i] = L[(i * 16 + a) * 16 + cc];
            dft16<-1>(xx);
#pragma unroll
            for (int q = 0; q < 16; ++q) xx[q] = cmul(xx[q], (cf){bfl(wpk[q]), bfh(wpk[q])});
            dft16<1>(xx);
#pragma unroll
            for (int i = 1; i < 16; ++i) xx[i] = cmul(xx[i], tw[i]);
#pragma unroll
            for (int i = 0; i < 16; ++i) L[(i * 16 + a) * 16 + cc] = xx[i];
            SYNC_S(t); ++t;
#pragma unroll
            for (int p = 0; p < 16; ++p) xx[p] = L[(a * 16 + p) * 16 + cc];
            dft16<1>(xx);
            if (ok) {
#pragma unroll
                for (int b = 0; b < 16; ++b) stc(rT2, tvo, tso + b * (16 * KP * 8), xx[b]); }
            wait_v<0>();
            if (lane == 0) flag_set(flags + F_B2 + task * 4 + wq, tag);
        }
        if (grp == 1) { for (; t <= qL; ++t) SYNC_S(t); } else { for (; t <= qL; ++t) BAR(); }
#undef STAGE_K
#undef STAGE_V
#undef SYNC
#undef SYNC_S
    }
    STAMPV(tsv, 40);
    {
        using namespace k3;
        St s; s.lds = lds;
        const int tid = (int)opaque(threadIdx.x), lane = tid & 63, wm = wid & 3, nh = wid >> 2, fr = lane & 15, fq = lane >> 4;
        const int sk = xcd >> 1, nt = idx & 3, mt = (pr >> 2) * 8 + (xcd & 1) * 4 + (pr & 3), tile = mt * 4 + nt;
        s.rA = mk_rsrc(ws + WS_CTX, (unsigned)TSZ); s.rB = mk_rsrc(wo, NQKV * DM * 2);
        s.sa = (unsigned)((mt * 4 * 256 + sk * 64) * 1024); s.sb = (unsigned)(sk * 1024 * DM * 2 + nt * 144 * 2);
        s.extra = wid < 2; s.ldsw = wid * 1024;
#pragma unroll
        for (int i = 0; i < 2; ++i) { const int b = tid * 16 + i * 8192, r = b >> 7, kc = ((b >> 4) & 7) ^ ((r >> 1) & 7); s.voffA[i] = (unsigned)(((r >> 5) * 128 + (kc >> 2)) * 2048 + (r & 31) * 64 + (kc & 3) * 16); }
#pragma unroll
        for (int i = 0; i < 3; ++i) { const int b = tid * 16 + i * 8192, rho = (b / 288) & 63, ch = (b % 288) >> 4;
            const int r5 = rho & 31, pos = (rho & 32) + 8 * ((r5 >> 2) & 3) + 4 * (r5 >> 4) + (r5 & 3);
            const int d = (pos & ~15) + 8 * ((pos >> 2) & 1) + 4 * ((pos >> 3) & 1) + (pos & 3);
            s.voffB[i] = (unsigned)(d * DM * 2 + ch * 16); }
        s.aoff[0] = lbase + (unsigned)((wm * 32 + fr) * 128 + ((fq ^ ((fr >> 1) & 7)) * 16)); s.aoff[1] = s.aoff[0] ^ 64u;
        s.boff = lbase + ABYTES + (unsigned)((4 * fq + ((lane & 15) >> 2)) * 288 + 8 * (lane & 3) + nh * 160);
        WAIT_L0(); BAR(); asm volatile("" ::: "memory");
        stageB(s, 0, 0); stageB(s, 1, 1); stageB(s, 2, 2); stageB(s, 3, 3);
        flag_wait(flags + F_CTX + (mt * 32 + sk * 8) * 4 + (lane & 31), tag, true);
        stageA(s, 0, 0); stageA(s, 1, 1); stageA(s, 2, 2); stageA(s, 3, 3);
        wait_v<6>(); BAR();
        STAMPV(tsv, 41);
        if (nh == 0) mainloop<5>(s); else mainloop<4>(s);
        STAMPV(tsv, 42);
        const rsrc_t rS = mk_rsrc(ws + WS_SLAB, 64 * 4 * 4 * 8 * 3 * SLABQ);
        const unsigned sbase = (unsigned)(tile * (4 * 4 * 8 * 3 * SLABQ) + wid * (3 * SLABQ));
        const int x8 = wm, x9 = (wm + 2) & 3;
#pragma unroll
        for (int q = 0; q < 10; ++q) {
            const int own = q < 8 ? (q & 3) : (q == 8 ? x8 : x9), slot = q >> 2;
            if (own != sk && (q < 8 || nh == 0)) bstore128c(__builtin_bit_cast(u32x4, s.acc[q & 1][q >> 1]), rS, lane * 16, sbase + (unsigned)((own * 4 + sk) * (8 * 3 * SLABQ) + slot * SLABQ));
        }
        flag_waitN<10>(flags + F_B2, tag, lane, 608);
        wait_v<0>();
        if (lane < 4 && lane != sk) flag_set(flags + F_SLAB + ((tile * 4 + lane) * 8 + wid) * 4 + sk, tag);
        STAMPV(tsv, 43);
        { fb::cf z[8];
          fb::b3_load(z, rT2, bid, tid);
          BAR(); asm volatile("" ::: "memory");
          fb::b3(z, out, (LAS fb::cf*)lds, bid, tid); }
        STAMPV(tsv, 44);
        flag_wait(flags + F_SLAB + ((tile * 4 + sk) * 8 + wid) * 4 + (lane & 3), tag, (lane & 3) != sk);
        STAMPV(tsv, 45);
        { f32x4 part[3][3];
          const bool has3 = nh == 0 && (x8 == sk || x9 == sk);
#pragma unroll
          for (int j = 0; j < 3; ++j) { const int src = (sk + 1 + j) & 3; const unsigned so = sbase + (unsigned)((sk * 4 + src) * (8 * 3 * SLABQ));
              part[j][0] = __builtin_bit_cast(f32x4, __builtin_amdgcn_raw_buffer_load_b128(rS, lane * 16, so, 16));
              part[j][1] = __builtin_bit_cast(f32x4, __builtin_amdgcn_raw_buffer_load_b128(rS, lane * 16, so + SLABQ, 16));
              part[j][2] = (f32x4){0.f, 0.f, 0.f, 0.f};
              if (has3) part[j][2] = __builtin_bit_cast(f32x4, __builtin_amdgcn_raw_buffer_load_b128(rS, lane * 16, so + 2 * SLABQ, 16)); }
          f32x4 mine[3];
          mine[0] = sk == 0 ? s.acc[0][0] : sk == 1 ? s.acc[1][0] : sk == 2 ? s.acc[0][1] : s.acc[1][1];
          mine[1] = sk == 0 ? s.acc[0][2] : sk == 1 ? s.acc[1][2] : sk == 2 ? s.acc[0][3] : s.acc[1][3];
          mine[2] = x8 == sk ? s.acc[0][4] : s.acc[1][4];
          u16* op = out + (size_t)(mt * 128 + wm * 32 + fr) * (2 * DM) + nt * 144 + nh * 80 + 4 * fq;
#pragma unroll
          for (int i = 0; i < 3; ++i) if (i < 2 || has3) {
              const f32x4 r = (mine[i] + part[0][i]) + (part[1][i] + part[2][i]);
              const int q = i < 2 ? sk + 4 * i : (x8 == sk ? 8 : 9);
              *(u32x2*)(op + (size_t)(q & 1) * 16 * (2 * DM) + 16 * (q >> 1)) = (u32x2){cvtpk(r[0], r[1]), cvtpk(r[2], r[3])};
          } }
    }
}

extern "C" void kernel_launch(void* const* d_in, const int* in_sizes, int n_in, void* d_out, int out_size, void* d_ws, size_t ws_size, hipStream_t stream) {
    (void)in_sizes; (void)n_in; (void)out_size; (void)ws_size;
    (void)hipFuncSetAttribute((const void*)fused, hipFuncAttributeMaxDynamicSharedMemorySize, 163840);
    fused<<<256, 512, 163840, stream>>>((const u16*)d_in[0], (const u16*)d_in[1], (const u16*)d_in[2], (const u16*)d_in[3], (const u16*)d_in[4], (const u16*)d_in[5],
                                        (const u16*)d_in[6], (const u16*)d_in[7], (const u16*)d_in[8], (u16*)d_out, (char*)d_ws);
}
```

```cpp
#include <hip/hip_runtime.h>
#include <stdint.h>
#include <utility>
typedef unsigned short u16;
typedef short bf16x8 __attribute__((ext_vector_type(8)));
typedef short s16x4 __attribute__((ext_vector_type(4)));
typedef float f32x2 __attribute__((ext_vector_type(2)));
typedef float f32x4 __attribute__((ext_vector_type(4)));
typedef float f32x16 __attribute__((ext_vector_type(16)));
typedef unsigned u32x4 __attribute__((ext_vector_type(4)));
typedef unsigned u32x2 __attribute__((ext_vector_type(2)));
typedef __bf16 bf16x2_t __attribute__((ext_vector_type(2)));
#define LAS __attribute__((address_space(3)))
#define DI __device__ __forceinline__
#define BAR() __builtin_amdgcn_s_barrier()
#define SCHED() __builtin_amdgcn_sched_barrier(0)
#define WAIT_L0() asm volatile("s_waitcnt lgkmcnt(0)" ::: "memory")
template <int N> DI void wait_v() { asm volatile("s_waitcnt vmcnt(%0)" ::"n"(N) : "memory"); }
DI unsigned cvtpk(float lo, float hi) { f32x2 v = {lo, hi}; bf16x2_t b = __builtin_convertvector(v, bf16x2_t); return __builtin_bit_cast(unsigned, b); }
DI bf16x8 lds_b128(const LAS unsigned char* p) { return *(const LAS bf16x8*)p; }
template <int OFF> DI s16x4 lds_tr(unsigned a) { s16x4 r; asm volatile("ds_read_b64_tr_b16 %0, %1 offset:%2" : "=v"(r) : "v"(a), "n"(OFF) : "memory"); return r; }
typedef __amdgpu_buffer_rsrc_t rsrc_t;
DI rsrc_t mk_rsrc(const void* p, unsigned bytes) { return __builtin_amdgcn_make_buffer_rsrc((void*)p, 0, bytes, 0x00020000); }
DI void blds16(rsrc_t r, LAS unsigned char* l, unsigned voff, unsigned soff) { __builtin_amdgcn_raw_ptr_buffer_load_lds(r, (LAS void*)l, 16, voff, soff, 0, 0); }
DI void blds16c(rsrc_t r, LAS unsigned char* l, unsigned voff, unsigned soff) { __builtin_amdgcn_raw_ptr_buffer_load_lds(r, (LAS void*)l, 16, voff, soff, 0, 16); }
DI void bstore128c(u32x4 v, rsrc_t r, unsigned voff, unsigned soff) { __builtin_amdgcn_raw_buffer_store_b128(v, r, voff, soff, 16); SCHED(); asm volatile("s_nop 1" ::: "memory"); SCHED(); }
typedef unsigned long long u64;
typedef __attribute__((address_space(1))) u64 gu64;
DI void flag_set(u64* p, u64 tag) { __hip_atomic_store((gu64*)p, tag, __ATOMIC_RELAXED, __HIP_MEMORY_SCOPE_AGENT); }
DI u64 flag_get(const u64* p) { return __hip_atomic_load((gu64*)p, __ATOMIC_RELAXED, __HIP_MEMORY_SCOPE_AGENT); }
DI void flag_wait(const u64* p, u64 tag, bool on) {
    for (unsigned spins = 0; spins < (1u << 20); ++spins) {
        const bool ok = !on || flag_get(p) == tag;
        if (__all(ok)) break;
        __builtin_amdgcn_s_sleep(6);
    }
    asm volatile("" ::: "memory");
}
template <int N> DI void flag_waitN(const u64* p, u64 tag, int lane, int count) {
    for (unsigned spins = 0; spins < (1u << 20); ++spins) {
        u64 v[N]; bool ok = true;
#pragma unroll
        for (int i = 0; i < N; ++i) { const int ix = lane + 64 * i; v[i] = flag_get(p + (ix < count ? ix : 0)); }
#pragma unroll
        for (int i = 0; i < N; ++i) ok &= v[i] == tag;
        if (__all(ok)) break;
        __builtin_amdgcn_s_sleep(6);
    }
    asm volatile("" ::: "memory");
}
DI unsigned opaque(unsigned x) { asm volatile("" : "+v"(x)); return x; }
DI int lane_id() { int l; asm volatile("v_mbcnt_lo_u32_b32 %0, -1, 0\n\tv_mbcnt_hi_u32_b32 %0, -1, %0" : "=v"(l)); return l; }
template <unsigned C> DI unsigned addc(unsigned x) { unsigned r; asm volatile("v_add_u32 %0, %1, %2" : "=v"(r) : "n"(C), "v"(x)); return r; }

#define STAMPV(tsv, n) do {} while (0)
#define STAMPR(tsv, n) do {} while (0)
#define STAMPOUT(base, tsv) do {} while (0)
constexpr int SEQ = 2048, NH = 32, HD = 128, DM = 576, DC = 512, NQKV = 4096;
constexpr float QSCALE = 0.08838834764831845f * 1.4426950408889634f;

constexpr size_t TSZ = (size_t)SEQ * NQKV * 2;
constexpr size_t WS_FLAG = 0, WS_K = 1 << 20, WS_V = WS_K + TSZ, WS_CTX = WS_V + TSZ, WS_SLAB = WS_CTX + TSZ, WS_T1 = WS_SLAB + (26u << 20), WS_T2 = WS_T1 + (5u << 20), WS_WP = WS_T2 + (5u << 20), WS_STAMP = (size_t)120 << 20;
constexpr int F_EPOCH = 0, F_B1 = 64, F_KVA = F_B1 + 256, F_KV = F_KVA + 32 * 64, F_CTX = F_KV + 32 * 64, F_B2 = F_CTX + 16 * 32 * 4, F_SLAB = F_B2 + 640;

namespace fb {
typedef f32x2 cf;
constexpr int KP = 304;
constexpr unsigned TBYTES = 8u * 256u * KP * 8u;
DI cf cmul(cf a, cf b) { return (cf){a.x * b.x - a.y * b.y, a.x * b.y + a.y * b.x}; }
DI cf cmulc(cf a, cf b) { return (cf){a.x * b.x + a.y * b.y, a.y * b.x - a.x * b.y}; }
DI cf conj(cf a) { return (cf){a.x, -a.y}; }
template <int S> DI cf muli(cf a) { return S > 0 ? (cf){-a.y, a.x} : (cf){a.y, -a.x}; }
DI cf twid(float rev) { return (cf){__builtin_amdgcn_cosf(rev), __builtin_amdgcn_sinf(rev)}; }
DI cf ldc(rsrc_t r, unsigned voff, unsigned soff) { return __builtin_bit_cast(cf, __builtin_amdgcn_raw_buffer_load_b64(r, voff, soff, 16)); }
DI void stc(rsrc_t r, unsigned voff, unsigned soff, cf v) { __builtin_amdgcn_raw_buffer_store_b64(__builtin_bit_cast(u32x2, v), r, voff, soff, 16); }
template <int S> DI void dft4(cf& a, cf& b, cf& c, cf& d) {
    const cf s0 = a + c, d0 = a - c, s1 = b + d, d1 = muli<S>(b - d);
    a = s0 + s1; c = s0 - s1; b = d0 + d1; d = d0 - d1;
}
template <int S> DI void dft8(cf (&x)[8]) {
    constexpr float R = 0.70710678118654752f;
    dft4<S>(x[0], x[2], x[4], x[6]); dft4<S>(x[1], x[3], x[5], x[7]);
    const cf o1 = (x[3] + muli<S>(x[3])) * R, o2 = muli<S>(x[5]), o3 = (muli<S>(x[7]) - x[7]) * R, o0 = x[1];
    const cf e0 = x[0], e1 = x[2], e2 = x[4], e3 = x[6];
    x[0] = e0 + o0; x[4] = e0 - o0; x[1] = e1 + o1; x[5] = e1 - o1; x[2] = e2 + o2; x[6] = e2 - o2; x[3] = e3 + o3; x[7] = e3 - o3;
}
template <int S> DI void dft3(cf& a, cf& b, cf& c) {
    constexpr float H = 0.86602540378443865f;
    const cf s = b + c, d = muli<S>(b - c) * H, m = a - s * 0.5f;
    a = a + s; b = m + d; c = m - d;
}
template <int S> DI void dft9(cf (&x)[9]) {
    constexpr float C1 = 0.76604444311897801f, S1 = 0.64278760968653933f, C2 = 0.17364817766693035f, S2 = 0.98480775301220806f, C4 = -0.93969262078590838f, S4 = 0.34202014332566873f;
    dft3<S>(x[0], x[3], x[6]); dft3<S>(x[1], x[4], x[7]); dft3<S>(x[2], x[5], x[8]);
    const cf w1 = {C1, S * S1}, w2 = {C2, S * S2}, w4 = {C4, S * S4};
    x[4] = cmul(x[4], w1); x[5] = cmul(x[5], w2); x[7] = cmul(x[7], w2); x[8] = cmul(x[8], w4);
    dft3<S>(x[0], x[1], x[2]); dft3<S>(x[3], x[4], x[5]); dft3<S>(x[6], x[7], x[8]);
    cf t;
    t = x[1]; x[1] = x[3]; x[3] = t; t = x[2]; x[2] = x[6]; x[6] = t; t = x[5]; x[5] = x[7]; x[7] = t;
}
template <int S> DI void dft16(cf (&x)[16]) {
    constexpr float C1 = 0.92387953251128674f, S1 = 0.38268343236508977f, R = 0.70710678118654752f;
#pragma unroll
    for (int n2 = 0; n2 < 4; ++n2) dft4<S>(x[n2], x[4 + n2], x[8 + n2], x[12 + n2]);
    const cf w1 = {C1, S * S1}, w2 = {R, S * R}, w3 = {S1, S * C1}, w6 = {-R, S * R}, w9 = {-C1, -S * S1};
    x[5] = cmul(x[5], w1); x[6] = cmul(x[6], w2); x[7] = cmul(x[7], w3);
    x[9] = cmul(x[9], w2); x[10] = muli<S>(x[10]); x[11] = cmul(x[11], w6);
    x[13] = cmul(x[13], w3); x[14] = cmul(x[14], w6); x[15] = cmul(x[15], w9);
#pragma unroll
    for (int k1 = 0; k1 < 4; ++k1) dft4<S>(x[4 * k1], x[4 * k1 + 1], x[4 * k1 + 2], x[4 * k1 + 3]);
#pragma unroll
    for (int a = 0; a < 4; ++a)
#pragma unroll
        for (int b = a + 1; b < 4; ++b) { const cf t = x[4 * a + b]; x[4 * a + b] = x[4 * b + a]; x[4 * b + a] = t; }
}
constexpr int SEQP = 648;
DI float bfl(unsigned w) { return __uint_as_float(w << 16); }
DI float bfh(unsigned w) { return __uint_as_float(w & 0xffff0000u); }
#define LBAR() do { WAIT_L0(); BAR(); asm volatile("" ::: "memory"); } while (0)
template <int S> DI void fft576x4(LAS cf* L, int tid) {
    if (tid < 256) {
        const int sq = tid >> 6, bc = tid & 63; LAS cf* p = L + sq * SEQP + bc; cf x[9];
#pragma unroll
        for (int a = 0; a < 9; ++a) x[a] = p[72 * a];
        dft9<S>(x);
#pragma unroll
        for (int k = 1; k < 9; ++k) x[k] = cmul(x[k], twid((float)(S * bc * k) * (1.0f / 576.0f)));
#pragma unroll
        for (int a = 0; a < 9; ++a) p[72 * a] = x[a];
    }
    LBAR();
    if (tid < 288) {
        const int sq = tid / 72, r = tid - sq * 72, k1 = r >> 3, c = r & 7; LAS cf* p = L + sq * SEQP + 72 * k1 + c; cf x[8];
#pragma unroll
        for (int b = 0; b < 8; ++b) x[b] = p[8 * b];
        dft8<S>(x);
#pragma unroll
        for (int k = 1; k < 8; ++k) x[k] = cmul(x[k], twid((float)(S * c * k) * (1.0f / 64.0f)));
#pragma unroll
        for (int b = 0; b < 8; ++b) p[8 * b] = x[b];
    }
    LBAR();
    if (tid < 288) {
        const int sq = tid / 72, r = tid - sq * 72, k1 = r >> 3, k2 = r & 7; LAS cf* p = L + sq * SEQP + 72 * k1 + 8 * k2; cf x[8];
#pragma unroll
        for (int c = 0; c < 8; ++c) x[c] = p[c];
        dft8<S>(x);
#pragma unroll
        for (int c = 0; c < 8; ++c) p[c] = x[c];
    }
    LBAR();
}
DI int pos_in(int d) { return 72 * (d >> 6) + (d & 63); }
DI int pos_out(int k) { const int k3 = k / 72, r = k - 72 * k3, k2 = r / 9, k1 = r - 9 * k2; return 72 * k1 + 8 * k2 + k3; }

DI void b1_pre(const unsigned (&w)[8], LAS cf* L, int tid) {
    if (tid < 288) {
        cf v[8];
#pragma unroll
        for (int m = 0; m < 8; ++m) v[m] = (cf){bfl(w[m]), bfh(w[m])};
        constexpr float R = 0.70710678118654752f;
        const cf a0 = v[0] + v[4], a1 = v[0] - v[4], a2 = v[2] + v[6], a3 = v[2] - v[6], b0 = v[1] + v[5], b1_ = v[1] - v[5], b2 = v[3] + v[7], b3 = v[3] - v[7];
        const cf g0 = (a0 + a2) + (b0 + b2), g4 = (a0 + a2) - (b0 + b2);
        const cf g2r = a0 - a2, g2i = b2 - b0;
        const cf p = (b1_ - b3) * R, q = (b1_ + b3) * R;
        const cf g1r = a1 + p, g1i = -(a3 + q), g3r = a1 - p, g3i = a3 - q;
        const int p0 = pos_in(2 * tid);
        *(LAS f32x4*)(L + 0 * SEQP + p0) = (f32x4){g0.x, g4.x, g0.y, g4.y};
        *(LAS f32x4*)(L + 1 * SEQP + p0) = (f32x4){g1r.x, g1i.x, g1r.y, g1i.y};
        *(LAS f32x4*)(L + 2 * SEQP + p0) = (f32x4){g2r.x, g2i.x, g2r.y, g2i.y};
        *(LAS f32x4*)(L + 3 * SEQP + p0) = (f32x4){g3r.x, g3i.x, g3r.y, g3i.y};
    }
}
DI void b1_post(rsrc_t rT1, LAS cf* L, int s0, int tid) {
    LBAR();
    fft576x4<-1>(L, tid);
    if (tid < 289) {
        const int k = tid, pa = pos_out(k), pb = pos_out(k == 0 ? 0 : 576 - k);
        cf t[8];
        { const cf a = L[pa], b = conj(L[pb]); t[0] = (a + b) * 0.5f; t[4] = muli<-1>(a - b) * 0.5f; }
#pragma unroll
        for (int j = 1; j < 4; ++j) { t[j] = L[j * SEQP + pa]; t[8 - j] = conj(L[j * SEQP + pb]); }
        const unsigned so = (unsigned)(s0 * KP * 8);
        stc(rT1, k * 8, so, t[0]);
#pragma unroll
        for (int j = 1; j < 8; ++j) stc(rT1, k * 8, so + j * (256 * KP * 8), cmul(t[j], twid((float)(-s0 * j) * (1.0f / 2048.0f))));
    }
    LBAR();
}
DI void b3_load(cf (&z)[8], rsrc_t rT2, int s0, int tid) {
    const int k = tid < 289 ? tid : 288;
#pragma unroll
    for (int j = 0; j < 8; ++j) z[j] = ldc(rT2, (unsigned)((k >> 2) * 8192 + (k & 3) * 8), (unsigned)(s0 * 32 + j * (76 * 8192)));
}
DI void b3(cf (&z)[8], u16* __restrict__ out, LAS cf* L, int s0, int tid) {
    if (tid < 289) {
        const int k = tid;
#pragma unroll
        for (int j = 1; j < 8; ++j) z[j] = cmul(z[j], twid((float)(s0 * j) * (1.0f / 2048.0f)));
        dft8<1>(z);
        const bool edge = (unsigned)(k - 1) >= 287u;
        const int pa = pos_in(k), pb = pos_in(576 - k);
#pragma unroll
        for (int c = 0; c < 4; ++c) {
            cf za = z[2 * c], zb = z[2 * c + 1];
            za.y = edge ? 0.f : za.y; zb.y = edge ? 0.f : zb.y;
            L[c * SEQP + pa] = (cf){za.x - zb.y, za.y + zb.x};
            if (!edge) L[c * SEQP + pb] = (cf){za.x + zb.y, zb.x - za.y};
        }
    }
    LBAR();
    fft576x4<1>(L, tid);
    if (tid < 288) {
        constexpr float SC = 1.0f / (2048.0f * 576.0f);
        const int p0 = pos_out(2 * tid), p1 = pos_out(2 * tid + 1);
#pragma unroll
        for (int c = 0; c < 4; ++c) {
            const cf a = L[c * SEQP + p0] * SC, b = L[c * SEQP + p1] * SC;
            *(unsigned*)(out + (size_t)(s0 + 256 * (2 * c)) * (2 * DM) + DM + 2 * tid) = cvtpk(a.x, b.x);
            *(unsigned*)(out + (size_t)(s0 + 256 * (2 * c + 1)) * (2 * DM) + DM + 2 * tid) = cvtpk(a.y, b.y);
        }
    }
}
}
namespace k1 {
constexpr int NS = 10, SLOT = 16384, NL = 59, NP = 50, NPRO = 8;
struct HT { int kind, kt, rd; };
constexpr HT ht(int i) {
    if (i < 32) { const int t = i >> 2, e = i & 3;
        return e == 0 ? HT{0, t, 4 * t + 1} : e == 1 ? HT{3, t, 4 * t + 1} : e == 2 ? HT{1, t, 4 * t + 2} : HT{4, t, 4 * t + 3}; }
    const int u = (i - 32) / 3, e = (i - 32) % 3;
    return e == 0 ? HT{2, u, 33 + 2 * u} : e == 1 ? HT{5, u, 33 + 2 * u} : HT{6, u, 34 + 2 * u};
}
constexpr int PKV = 43;
constexpr int PPFE = 12;
constexpr int nstores(int p) { return ((p >= 30 && p <= 45) ? 1 : 0) + (p == PKV ? 1 : 0); }
struct Sched { int issue[NL]; int wait[NP + 1]; int first[NP + 2]; int kvcov; };
constexpr Sched make_sched() {
    Sched s{}; int cnt[NP + 2] = {};
    for (int i = 0; i < NL; ++i) {
        int e = i < NPRO ? 0 : 1;
        if (i >= NS && ht(i - NS).rd + 2 > e) e = ht(i - NS).rd + 2;
        if (i > 0 && s.issue[i - 1] > e) e = s.issue[i - 1];
        while (e > 0 && cnt[e] >= 2) ++e;
        s.issue[i] = e; cnt[e]++;
    }
    int endpos[NL] = {}; int tot = 0, li = 0, prev_last = -1, kvend = 1 << 30; s.kvcov = 1 << 30;
    for (int p = 0; p <= NP; ++p) {
        s.first[p] = li;
        tot += nstores(p);
        if (p == 37) kvend = tot;
        while (li < NL && s.issue[li] == p) { tot += 2; endpos[li] = tot; ++li; }
        int last = -1; for (int i = 0; i < NL; ++i) if (ht(i).rd <= p + 1) last = i;
        if (last > prev_last) { s.wait[p] = (s.issue[last] <= p) ? tot - endpos[last] : -1000; prev_last = last;
            if (s.wait[p] >= 0 && tot - s.wait[p] >= kvend && s.kvcov > p) s.kvcov = p; } else s.wait[p] = -1;
    }
    s.first[NP + 1] = li;
    return s;
}
constexpr Sched SC = make_sched();
constexpr bool sched_ok() { for (int p = 0; p <= NP; ++p) if (SC.wait[p] < -1 || SC.wait[p] > 63) return false; return SC.first[NP + 1] == NL; }
static_assert(sched_ok(), "schedule");
static_assert(SC.kvcov < PKV, "the KV stores must be covered by a wait before the flag");

struct St {
    LAS unsigned char* lds; unsigned tsv;
    rsrc_t rw[3], ra[2]; unsigned sw, sa[3];
    u64 tag; u64 *fkv; int wid, idx; unsigned pf[2], swx;
    unsigned voffW[2], voffC[2], voffX[2];
    unsigned ldsw, aoff, boff[2];
    f32x4 acc[2][2][4][2];
    bf16x8 At[4][2]; s16x4 B0[2][2][2], B1[2][2][2];
    u32x4 pk[16]; rsrc_t rK, rV; unsigned evo, skq, sv;
};
template <int I> DI void stage(St& s) {
    constexpr HT h = ht(I); constexpr int slot = I % NS;
    LAS unsigned char* dst = s.lds + slot * SLOT + s.ldsw;
    if constexpr (h.kind <= 2) { const unsigned so = s.sw + h.kt * (64 * NQKV * 2); blds16(s.rw[h.kind], dst, s.voffW[0], so); blds16(s.rw[h.kind], dst + 8192, s.voffW[1], so); }
    else if constexpr (h.kind <= 4) { const unsigned so = s.sa[0] + (h.kind - 3) * (128 * DC * 2) + h.kt * 128; blds16(s.ra[0], dst, s.voffC[0], so); blds16(s.ra[0], dst + 8192, s.voffC[1], so); }
    else { const unsigned so = s.sa[h.kind - 4] + h.kt * 128; blds16(s.ra[1], dst, s.voffX[0], so); blds16(s.ra[1], dst + 8192, s.voffX[1], so); }
}
template <int I> DI void lda(St& s) {
    constexpr int slot = I % NS;
    const unsigned a0 = addc<slot * SLOT>(s.aoff), a1 = a0 ^ 64u;
#pragma unroll
    for (int m = 0; m < 4; ++m) { s.At[m][0] = lds_b128((const LAS unsigned char*)a0 + m * 2048); s.At[m][1] = lds_b128((const LAS unsigned char*)a1 + m * 2048); }
}
template <int I> DI void ldb(St& s, s16x4 (&B)[2][2][2]) {
    constexpr int slot = I % NS;
#pragma unroll
    for (int n = 0; n < 2; ++n) {
        const unsigned p = addc<slot * SLOT>(s.boff[n]);
        B[n][0][0] = lds_tr<0>(p); B[n][0][1] = lds_tr<1024>(p); B[n][1][0] = lds_tr<8192>(p); B[n][1][1] = lds_tr<9216>(p);
    }
}
template <bool TR, bool ZERO> DI void mma(f32x4 (&acc)[4][2], const bf16x8 (&At)[4][2], const s16x4 (&Bh)[2][2][2]) {
    bf16x8 B[2][2];
#pragma unroll
    for (int n = 0; n < 2; ++n)
#pragma unroll
        for (int k = 0; k < 2; ++k) B[n][k] = __builtin_shufflevector(Bh[n][k][0], Bh[n][k][1], 0, 1, 2, 3, 4, 5, 6, 7);
    __builtin_amdgcn_s_setprio(1);
#pragma unroll
    for (int k = 0; k < 2; ++k)
#pragma unroll
        for (int m = 0; m < 4; ++m)
#pragma unroll
            for (int n = 0; n < 2; ++n) {
                const f32x4 c = (ZERO && k == 0) ? (f32x4){0.f, 0.f, 0.f, 0.f} : acc[m][n];
                acc[m][n] = TR ? __builtin_amdgcn_mfma_f32_16x16x32_bf16(B[n][k], At[m][k], c, 0, 0, 0)
                               : __builtin_amdgcn_mfma_f32_16x16x32_bf16(At[m][k], B[n][k], c, 0, 0, 0);
            }
    __builtin_amdgcn_s_setprio(0);
}
template <bool SCALE> DI void pack_t(const f32x4 (&a)[4][2], u32x4* w) {
#pragma unroll
    for (int m = 0; m < 4; ++m) {
        f32x4 v0 = a[m][0], v1 = a[m][1];
        if (SCALE) { v0 *= QSCALE; v1 *= QSCALE; }
        w[m] = (u32x4){cvtpk(v0[0], v0[1]), cvtpk(v0[2], v0[3]), cvtpk(v1[0], v1[1]), cvtpk(v1[2], v1[3])};
        asm volatile("" : "+v"(w[m]));
    }
}
DI void pack_v(const f32x4 (&a)[4][2], u32x4* w) {
#pragma unroll
    for (int u = 0; u < 2; ++u)
#pragma unroll
        for (int n = 0; n < 2; ++n) {
            const f32x4 v0 = a[2 * u][n], v1 = a[2 * u + 1][n];
            w[2 * u + n] = (u32x4){cvtpk(v0[0], v0[1]), cvtpk(v0[2], v0[3]), cvtpk(v1[0], v1[1]), cvtpk(v1[2], v1[3])};
            asm volatile("" : "+v"(w[2 * u + n]));
        }
}
template <int I> DI void store1(St& s) {
    constexpr int qd = I >> 2, i = I & 3, ai = qd >> 1;
    if constexpr ((qd & 1) == 0) bstore128c(s.pk[I], s.rK, s.evo, s.skq + (ai * 8 + i) * 4096);
    else bstore128c(s.pk[I], s.rV, s.evo, s.sv + (i & 1) * 65536 + (ai * 4 + (i >> 1)) * 1024);
}
DI void mk_voffx(St& s) {
    const int tid = s.wid * 64 + lane_id();
#pragma unroll
    for (int i = 0; i < 2; ++i) { const int b = tid * 16 + i * 8192, r = b >> 7, kc = ((b >> 4) & 7) ^ ((r >> 1) & 7), pr = (r & ~24) | ((r & 8) << 1) | ((r & 16) >> 1);
        s.voffX[i] = (unsigned)(pr * DM * 2 + kc * 16); }
}
DI void warm_q(St& s) {
    const int l = lane_id(), e = (s.wid - 5) * 64 + l, ec = e < 144 ? e : 143;
    s.pf[0] = 0;
    if (s.wid == 3) asm volatile("buffer_load_dword %0, %1, %2, %3 offen" : "=v"(s.pf[0]) : "v"((unsigned)l * 128u), "s"(s.ra[1]), "s"((unsigned)(blockIdx.x * 8 * DM * 2)) : "memory");
    else if (s.wid == 4) asm volatile("buffer_load_dword %0, %1, %2, %3 offen" : "=v"(s.pf[0]) : "v"((unsigned)(64 + (l & 7)) * 128u), "s"(s.ra[1]), "s"((unsigned)(blockIdx.x * 8 * DM * 2)) : "memory");
    else if (s.wid >= 5) asm volatile("buffer_load_dword %0, %1, %2, %3 offen" : "=v"(s.pf[0]) : "v"((unsigned)((ec >> 3) * (NQKV * 2) + (ec & 7) * 128)), "s"(s.rw[2]), "s"(s.swx + (unsigned)(s.idx * 18 * NQKV * 2)) : "memory");
}
template <int A, int B> DI void stage_range(St& s) { if constexpr (A < B) { stage<A>(s); stage_range<A + 1, B>(s); } }
template <int P> DI void phase(St& s) {
    if constexpr (P <= 32) {
        constexpr int t = (P - 1) >> 2, e = (P - 1) & 3;
        if constexpr (e == 0) { ldb<4 * t>(s, s.B0); lda<4 * t + 1>(s); }
        if constexpr (e == 1) ldb<4 * t + 2>(s, s.B1);
        if constexpr (e == 2) lda<4 * t + 3>(s);
    } else {
        constexpr int u = (P - 33) >> 1, e = (P - 33) & 1;
        if constexpr (e == 0) { ldb<32 + 3 * u>(s, s.B0); lda<33 + 3 * u>(s); }
        else lda<34 + 3 * u>(s);
    }
    if constexpr (P == 30) { const int l_ = lane_id(); s.evo = (unsigned)((l_ & 15) * 64 + (l_ >> 4) * 16); pack_t<false>(s.acc[0][0], s.pk); }
    if constexpr (P == 31) pack_v(s.acc[0][1], s.pk + 4);
    if constexpr (P == 32) pack_t<false>(s.acc[1][0], s.pk + 8);
    if constexpr (P == 33) pack_v(s.acc[1][1], s.pk + 12);
    if constexpr (P >= 30 && P <= 45) store1<P - 30>(s);
    if constexpr (P == PKV) { if (lane_id() == 0) flag_set(s.fkv, s.tag); }
    if constexpr (P == PPFE) asm volatile("" :: "v"(s.pf[0]));
    if constexpr (P == SC.issue[33]) mk_voffx(s);
    stage_range<SC.first[P], SC.first[P + 1]>(s);
    if constexpr (SC.wait[P] >= 0) wait_v<SC.wait[P]>();
    BAR(); WAIT_L0(); SCHED();
    if constexpr (P <= 32) {
        constexpr int t = (P - 1) >> 2, e = (P - 1) & 3;
        if constexpr (e == 0) mma<true, t == 0>(s.acc[0][0], s.At, s.B0);
        if constexpr (e == 1) mma<false, t == 0>(s.acc[0][1], s.At, s.B1);
        if constexpr (e == 2) mma<true, t == 0>(s.acc[1][0], s.At, s.B0);
        if constexpr (e == 3) mma<false, t == 0>(s.acc[1][1], s.At, s.B1);
    } else {
        constexpr int u = (P - 33) >> 1, e = (P - 33) & 1;
        mma<true, u == 0>(s.acc[e][0], s.At, s.B0);
    }
    BAR(); SCHED();
    if constexpr (P % 5 == 0) STAMPV(s.tsv, 2 + P / 5);
}
template <int... Ps> DI void phases(St& s, std::integer_sequence<int, Ps...>) { (phase<Ps + 1>(s), ...); }
}

namespace k2 {
constexpr int UNIT = 65536, VOFF = 32768, XTRA = 131072;
constexpr float THR = 8.0f;
#define K2_PD 4
constexpr int PD = K2_PD;
#define MFMA32(a, b, c) __builtin_amdgcn_mfma_f32_32x32x16_bf16((a), (b), (c), 0, 0, 0)
#define PIN(x) asm volatile("" : "+v"(x))
DI float max2f(float a, float b) { return __builtin_elementwise_maximum(a, b); }
DI float max3f(float a, float b, float c) { return max2f(max2f(a, b), c); }
DI float swap_max(float x) { auto r = __builtin_amdgcn_permlane32_swap(__float_as_uint(x), __float_as_uint(x), false, false); return max2f(__uint_as_float(r[0]), __uint_as_float(r[1])); }
DI float swap_add(float x) { auto r = __builtin_amdgcn_permlane32_swap(__float_as_uint(x), __float_as_uint(x), false, false); return __uint_as_float(r[0]) + __uint_as_float(r[1]); }
DI void mask16(f32x16& s, int dq) {
    const float NEG = -__builtin_inff();
#pragma unroll
    for (int r = 0; r < 16; ++r) { const int c = (r & 3) + 8 * (r >> 2); s[r] = c > dq ? NEG : s[r]; }
}
struct St {
    unsigned ka[8];
    bf16x8 qr[8], fr[16 + 8]; u32x4 p[2][2];
    f32x16 o[4], s[2], negm;
    float l0, l1, e[16], mxa, mxb; int dq;
};
template <int I, int POS, bool NOPV> DI void ldfrag(St& c) {
    if constexpr (I < 8) c.fr[I] = lds_b128((const LAS unsigned char*)c.ka[I] + POS * 8192);
    else if constexpr (I < 16) { if constexpr (!NOPV) c.fr[I] = lds_b128((const LAS unsigned char*)c.ka[2 * POS + ((I - 8) >> 2)] + VOFF + ((I - 8) & 3) * 8192); }
    else if constexpr (POS < 3) c.fr[I] = lds_b128((const LAS unsigned char*)c.ka[I - 16] + (POS + 1) * 8192);
}
template <int X, int E> DI void do_exp(St& c) { c.e[E] = __builtin_amdgcn_exp2f(c.s[X][E]); PIN(c.e[E]); }
template <int X, int E> DI void do_fin(St& c) {
    if constexpr (E & 1) { c.l1 += c.e[E]; PIN(c.l1); unsigned w = cvtpk(c.e[E - 1], c.e[E]); PIN(w); c.p[X][E >> 3][(E >> 1) & 3] = w; }
    else { c.l0 += c.e[E]; PIN(c.l0); }
}
template <int X, int I> DI void do_max(St& c) {
    const f32x16& v = c.s[X];
    if constexpr (I == 0) c.mxa = max2f(v[0], v[1]);
    else if constexpr (I == 1) c.mxb = max2f(v[2], v[3]);
    else if constexpr (I & 1) c.mxb = max3f(c.mxb, v[2 * I], v[2 * I + 1]);
    else c.mxa = max3f(c.mxa, v[2 * I], v[2 * I + 1]);
    if constexpr (I & 1) PIN(c.mxb); else PIN(c.mxa);
}
template <int G, int POS, bool NOPV> DI void gapA(St& c) {
    constexpr int X = POS & 1;
    c.s[X] = MFMA32(c.fr[G], c.qr[G], G == 0 ? c.negm : c.s[X]);
    ldfrag<G + PD, POS, NOPV>(c);
    if constexpr (G >= 1) do_fin<X ^ 1, G - 1>(c);
    do_exp<X ^ 1, G>(c);
    SCHED();
}
template <int G, int POS, bool NOPV> DI void gapB(St& c) {
    constexpr int X = POS & 1, ks = G >> 2, db = G & 3;
    if constexpr (!NOPV) c.o[db] = MFMA32(c.fr[8 + G], __builtin_bit_cast(bf16x8, c.p[X][ks]), c.o[db]);
    ldfrag<8 + G + PD, POS, NOPV>(c);
    if constexpr (G == 0) { do_fin<X ^ 1, 7>(c); do_exp<X ^ 1, 8>(c); do_exp<X ^ 1, 9>(c); }
    if constexpr (G == 1) { do_fin<X ^ 1, 8>(c); do_fin<X ^ 1, 9>(c); do_exp<X ^ 1, 10>(c); }
    if constexpr (G >= 2) do_fin<X ^ 1, 8 + G>(c);
    if constexpr (G >= 2 && G <= 6) do_exp<X ^ 1, 9 + G>(c);
    if constexpr (G == 2) { do_max<X, 0>(c); do_max<X, 1>(c); }
    if constexpr (G == 3) { do_max<X, 2>(c); }
    if constexpr (G == 4) { do_max<X, 3>(c); do_max<X, 4>(c); }
    if constexpr (G == 5) { do_max<X, 5>(c); }
    if constexpr (G == 6) { do_max<X, 6>(c); do_max<X, 7>(c); }
    if constexpr (G == 7) { c.mxa = max2f(c.mxa, c.mxb); PIN(c.mxa); }
    SCHED();
}
template <int POS, bool NOPV, int O, int... Gs> DI void phaseA(St& c, std::integer_sequence<int, Gs...>) { (gapA<Gs + O, POS, NOPV>(c), ...); }
template <int POS, bool NOPV, int O, int... Gs> DI void phaseB(St& c, std::integer_sequence<int, Gs...>) { (gapB<Gs + O, POS, NOPV>(c), ...); }
template <int POS, int... Is> DI void ldfirst(St& c, std::integer_sequence<int, Is...>) { (ldfrag<Is, POS, false>(c), ...); }
template <int... Is> DI void takefirst(St& c, std::integer_sequence<int, Is...>) { ((c.fr[Is] = c.fr[16 + Is]), ...); }
template <int X, int O, int... Es> DI void exps(St& c, std::integer_sequence<int, Es...>) { (do_exp<X, Es + O>(c), ...); }
template <int X, int O, int... Es> DI void fins(St& c, std::integer_sequence<int, Es...>) { (do_fin<X, Es + O>(c), ...); }
#define ISEQ(n) std::make_integer_sequence<int, n>{}
DI void rescale_o(St& c, float f) {
#pragma unroll
    for (int b = 0; b < 4; ++b)
#pragma unroll
        for (int r = 0; r < 16; ++r) c.o[b][r] *= f;
}
DI float max16(const f32x16& a) {
    float m0 = max3f(a[0], a[1], a[2]), m1 = max3f(a[3], a[4], a[5]);
    m0 = max3f(m0, a[6], a[7]); m1 = max3f(m1, a[8], a[9]); m0 = max3f(m0, a[10], a[11]); m1 = max3f(m1, a[12], a[13]);
    return max3f(m0, m1, max2f(a[14], a[15]));
}
template <int POS, bool NOPV, bool FIRST> DI void halfstep(St& c, bool diag) {
    constexpr int X = POS & 1;
    if constexpr (POS == 0) { ldfirst<0>(c, ISEQ(PD)); SCHED(); } else takefirst(c, ISEQ(PD));
    phaseA<POS, NOPV, 0>(c, ISEQ(8));
    phaseB<POS, NOPV, 0>(c, ISEQ(8));
    asm volatile("" ::: "memory");
    const float thr = diag ? -__builtin_inff() : THR;
    if (FIRST || __builtin_expect(__any(c.mxa > thr), 0)) {
        mask16(c.s[X], diag ? c.dq - 32 * POS : 64);
        const float mx = swap_max(max16(c.s[X]));
        if (FIRST || __any(mx > THR)) {
            const float dl = FIRST ? mx : max2f(mx, 0.f);
            if constexpr (!FIRST) {
                const float f = __builtin_amdgcn_exp2f(-dl);
                c.l0 *= f; c.l1 *= f; rescale_o(c, f);
#pragma unroll
                for (int i = 0; i < 8; ++i) { const unsigned w = c.p[X ^ 1][i >> 2][i & 3]; c.p[X ^ 1][i >> 2][i & 3] = cvtpk(__uint_as_float(w << 16) * f, __uint_as_float(w & 0xffff0000u) * f); }
            }
#pragma unroll
            for (int r = 0; r < 16; ++r) { c.s[X][r] -= dl; c.negm[r] -= dl; }
        }
    }
}
DI void pv_tail(St& c, const u32x4 (&p)[2], unsigned abase, int pos) {
#pragma unroll
    for (int ks = 0; ks < 2; ++ks) {
        const unsigned a = abase ^ (unsigned)((2 * pos + ks) << 5);
        bf16x8 f[4];
#pragma unroll
        for (int b = 0; b < 4; ++b) f[b] = lds_b128((const LAS unsigned char*)a + VOFF + b * 8192);
#pragma unroll
        for (int b = 0; b < 4; ++b) c.o[b] = MFMA32(f[b], __builtin_bit_cast(bf16x8, p[ks]), c.o[b]);
    }
}
}

namespace k3 {
constexpr int ABYTES = 16384, BBYTES = 18432, STG = ABYTES + BBYTES, NKT = 16, NSTG = 4;
constexpr int SLABQ = 1024;
struct St {
    LAS unsigned char* lds; unsigned tsv; rsrc_t rA, rB; unsigned voffA[2], voffB[3], sa, sb, ldsw; bool extra;
    unsigned aoff[2], boff;
    f32x4 acc[2][5];
    bf16x8 af[2][2]; s16x4 bf[2][5][2];
};
DI void stageA(St& s, int buf, int Tc) {
    LAS unsigned char* d = s.lds + buf * STG + s.ldsw;
    blds16c(s.rA, d, s.voffA[0], s.sa + Tc * 4096); blds16c(s.rA, d + 8192, s.voffA[1], s.sa + Tc * 4096);
}
DI void stageB(St& s, int buf, int Tc) {
    LAS unsigned char* d = s.lds + buf * STG + s.ldsw;
    blds16(s.rB, d + ABYTES, s.voffB[0], s.sb + Tc * (64 * DM * 2)); blds16(s.rB, d + ABYTES + 8192, s.voffB[1], s.sb + Tc * (64 * DM * 2));
    if (s.extra) blds16(s.rB, d + ABYTES + 16384, s.voffB[2], s.sb + Tc * (64 * DM * 2));
}
DI void stage(St& s, int buf, int Tc) { stageA(s, buf, Tc); stageB(s, buf, Tc); }
template <int I, int NC, int BUF, int KS, int X> DI void ldone(St& s, unsigned a, unsigned b) {
    if constexpr (I < 2) s.af[X][I] = lds_b128((const LAS unsigned char*)a + I * 2048);
    else if constexpr (I < 2 + 2 * NC) { constexpr int c = (I - 2) >> 1, hf = (I - 2) & 1; s.bf[X][c][hf] = lds_tr<c * 32 + hf * 16 * 288 + KS * 32 * 288>(b); }
}
template <int M, int NC, int BUF, int KS, int X, bool ZERO> DI void gap(St& s, unsigned a, unsigned b) {
    constexpr int c = M >> 1, m = M & 1;
    const bf16x8 bb = __builtin_shufflevector(s.bf[X][c][0], s.bf[X][c][1], 0, 1, 2, 3, 4, 5, 6, 7);
    s.acc[m][c] = __builtin_amdgcn_mfma_f32_16x16x32_bf16(bb, s.af[X][m], ZERO ? (f32x4){0.f, 0.f, 0.f, 0.f} : s.acc[m][c], 0, 0, 0);
    ldone<2 * M, NC, BUF, KS, X ^ 1>(s, a, b); ldone<2 * M + 1, NC, BUF, KS, X ^ 1>(s, a, b);
    SCHED();
}
template <int NC, int BUF, int KS, int X, bool ZERO, int... Ms> DI void kstep(St& s, std::integer_sequence<int, Ms...>) {
    const unsigned a = opaque(s.aoff[KS] + BUF * STG), b = s.boff + BUF * STG;
    (gap<Ms, NC, BUF, KS, X, ZERO>(s, a, b), ...);
    WAIT_L0(); SCHED();
}
template <int N> DI void waitv2(bool extra) { if (extra) wait_v<N * 5>(); else wait_v<N * 4>(); }
template <int NC, int B0, int W = 2, bool STG_ = true, int FIX = -1> DI void period(St& s, int T) {
    constexpr int B1 = (B0 + 1) & 3;
    if constexpr (FIX >= 0) wait_v<FIX>(); else waitv2<W>(s.extra);
    BAR(); SCHED();
    if constexpr (STG_) stage(s, B0, T + 4);
    SCHED();
    kstep<NC, B1, 0, 1, false>(s, std::make_integer_sequence<int, 2 * NC>{});
    kstep<NC, B1, 1, 0, false>(s, std::make_integer_sequence<int, 2 * NC>{});
}
template <int NC, int... Is> DI void ldall(St& s, std::integer_sequence<int, Is...>) { const unsigned a = opaque(s.aoff[0]), b = s.boff; (ldone<Is, NC, 0, 0, 0>(s, a, b), ...); }
template <int NC> DI void mainloop(St& s) {
    ldall<NC>(s, std::make_integer_sequence<int, 2 + 2 * NC>{});
    WAIT_L0(); SCHED();
    kstep<NC, 0, 1, 0, true>(s, std::make_integer_sequence<int, 2 * NC>{});
    period<NC, 0, 2, true, 4>(s, 0); period<NC, 1, 2, true, 6>(s, 1); period<NC, 2>(s, 2); period<NC, 3>(s, 3);
    for (int T = 4; T < 12; T += 4) { period<NC, 0>(s, T); period<NC, 1>(s, T + 1); period<NC, 2>(s, T + 2); period<NC, 3>(s, T + 3); }
    period<NC, 0, 2, false>(s, 12); period<NC, 1, 1, false>(s, 13); period<NC, 2, 0, false>(s, 14);
#pragma unroll
    for (int c = 0; c < NC; ++c) {
        const bf16x8 bb = __builtin_shufflevector(s.bf[1][c][0], s.bf[1][c][1], 0, 1, 2, 3, 4, 5, 6, 7);
#pragma unroll
        for (int m = 0; m < 2; ++m) s.acc[m][c] = __builtin_amdgcn_mfma_f32_16x16x32_bf16(bb, s.af[1][m], s.acc[m][c], 0, 0, 0);
    }
}
}

struct Params { const u16 *x, *c, *wq, *wuk, *wuv, *wo, *bx, *wr, *wi; u16* out; char* ws; };

__global__ __launch_bounds__(512, 2) void fused(const u16* __restrict__ x, const u16* __restrict__ cin, const u16* __restrict__ wq_, const u16* __restrict__ wuk, const u16* __restrict__ wuv,
                                                const u16* __restrict__ wo, const u16* __restrict__ bx, const u16* __restrict__ wr_, const u16* __restrict__ wi_, u16* __restrict__ out, char* __restrict__ ws) {
    extern __shared__ __attribute__((aligned(16))) unsigned char smem[];
    LAS unsigned char* lds = (LAS unsigned char*)smem;
    const unsigned lbase = (unsigned)(uintptr_t)lds;
    const int bid = blockIdx.x, xcd = bid & 7, idx = bid >> 3, h = xcd * 4 + (idx & 3), pr = idx >> 2, qL = 15 - pr;
    const int wid = __builtin_amdgcn_readfirstlane(threadIdx.x >> 6);
    unsigned tsv = 0x7f7f7f7f; STAMPV(tsv, 0); STAMPR(tsv, 62);
    const int s0 = xcd * 32 + idx;
    u64* const flags = (u64*)(ws + WS_FLAG);
    const rsrc_t rK = mk_rsrc(ws + WS_K, (unsigned)TSZ), rV = mk_rsrc(ws + WS_V, (unsigned)TSZ);
    const rsrc_t rT1 = mk_rsrc(ws + WS_T1, fb::TBYTES), rT2 = mk_rsrc(ws + WS_T2, fb::TBYTES);
    u64 tag; u64* pub = nullptr;
    k2::St c;
    {
        using namespace k1;
        St s; s.lds = lds; s.tsv = tsv; s.wid = wid; s.idx = idx; s.swx = (unsigned)(xcd * 4 * HD * 2);
        const int tid = threadIdx.x, lane = tid & 63, wr = wid >> 2, wc = wid & 3, fr = lane & 15, fq = lane >> 4;
        const u64 ep = flag_get(flags + F_EPOCH);
        unsigned bw[8];
        { const int t2 = tid < 288 ? tid : 287;
#pragma unroll
          for (int m = 0; m < 8; ++m) bw[m] = *(const unsigned*)(bx + (size_t)(s0 + 256 * m) * DM + 2 * t2); }
        s.rw[0] = mk_rsrc(wuk, DC * NQKV * 2); s.rw[1] = mk_rsrc(wuv, DC * NQKV * 2); s.rw[2] = mk_rsrc(wq_, DM * NQKV * 2);
        s.ra[0] = mk_rsrc(cin, SEQ * DC * 2); s.ra[1] = mk_rsrc(x, SEQ * DM * 2);
        s.sw = h * HD * 2; s.sa[0] = pr * 256 * DC * 2; s.sa[1] = qL * 128 * DM * 2; s.sa[2] = pr * 128 * DM * 2;
        s.rK = rK; s.rV = rV;
        s.skq = (unsigned)(((h * 128 + pr * 16 + wr * 4) * 4 + wc) * 1024);
        s.sv = (unsigned)(((h * 8 + 2 * wc) * 64 + pr * 8 + wr * 2) * 1024);
        s.fkv = flags + F_KVA + h * 64 + pr * 8 + wid;
#pragma unroll
        for (int i = 0; i < 2; ++i) {
            const int b = tid * 16 + i * 8192;
            { const int k = b >> 8, u = (b >> 5) & 7, hf = (b >> 4) & 1, f = (k & 3) | (((k >> 3) & 1) << 2); s.voffW[i] = (unsigned)(k * NQKV * 2 + ((u ^ f) * 16 + hf * 8) * 2); }
            { const int r = b >> 7, kc = ((b >> 4) & 7) ^ ((r >> 1) & 7), prr = (r & ~24) | ((r & 8) << 1) | ((r & 16) >> 1);
              s.voffC[i] = (unsigned)(prr * DC * 2 + kc * 16); }
        }
        s.ldsw = wid * 1024;
        stage_range<0, NPRO>(s);
        SCHED();
        tag = (((u64)__builtin_amdgcn_readfirstlane((unsigned)(ep >> 32)) << 32) | (u64)__builtin_amdgcn_readfirstlane((unsigned)ep)) + 1ull; s.tag = tag;
        fb::b1_pre(bw, (LAS fb::cf*)(lds + 8 * SLOT), tid);
        warm_q(s);
        fb::b1_post(rT1, (LAS fb::cf*)(lds + 8 * SLOT), s0, tid);
        s.aoff = lbase + (unsigned)((wr * 64 + fr) * 128 + ((fq ^ ((fr >> 1) & 7)) * 16));
        { const int q = (lane & 15) >> 2, p = lane & 3, f = q | ((fq & 1) << 2);
#pragma unroll
          for (int n = 0; n < 2; ++n) s.boff[n] = lbase + (unsigned)(256 * (8 * fq + q) + 32 * ((2 * wc + n) ^ f) + 8 * p); }
        wait_v<SC.wait[0]>();
        BAR();
        if (wr == 1) BAR();
        STAMPV(s.tsv, 1);
        phases(s, std::make_integer_sequence<int, NP>{});
        if (lane_id() == 0) flag_set(flags + F_KV + h * 64 + pr * 8 + wid, tag);
        if (wr == 0) BAR();
        if (wid == 0 && lane_id() == 0) flag_set(flags + F_B1 + bid, tag);
        pack_t<true>(s.acc[0][0], s.pk); pack_t<true>(s.acc[1][0], s.pk + 4);
        { const int l_ = lane_id(), fr_ = l_ & 15, fq_ = l_ >> 4;
#pragma unroll
          for (int i = 0; i < 8; ++i) { const int m = i & 3, row = wr * 64 + 32 * (m >> 1) + 16 * (fr_ >> 3) + 8 * (m & 1) + (fr_ & 7);
              *(LAS u32x4*)(lds + 98304 + (i >> 2) * 32768 + row * 256 + (((wc * 4 + fq_) ^ (row & 15)) << 4)) = s.pk[i]; } }
        tsv = s.tsv;
    }
    STAMPV(tsv, 13); STAMPR(tsv, 59);
    {
        using namespace k2;
        const int lane = lane_id(), tid = wid * 64 + lane, wq = wid & 3, grp = wid >> 2, r32 = lane & 31, hi = lane >> 5;
        const int qb = grp == 0 ? qL : pr;
        const unsigned hb = (unsigned)h * (SEQ * HD * 2);
        const int a0 = wq, srow = 4 * a0 + (lane >> 4), sch = (lane & 15) ^ srow;
        const unsigned voffK = (unsigned)((sch >> 2) * 1024 + (4 * (a0 & 1) + (lane >> 4)) * 64 + (sch & 3) * 16 + (a0 >> 1) * 4096);
        const unsigned voffV = (unsigned)((sch >> 2) * 1024 + srow * 64 + (sch & 3) * 16);
        const unsigned ldsw = (unsigned)(4 * a0 * 256);
        int nst = 16;
#define STAGE_K(t_) do { _Pragma("unroll") for (int j_ = 0; j_ < 8; ++j_) blds16c(rK, lds + ((t_) & 1) * UNIT + ldsw + j_ * 4096, voffK, hb + (unsigned)(t_) * 32768u + (j_ >> 1) * 8192 + (j_ & 1) * 512); } while (0)
#define STAGE_V(t_) do { const unsigned vo_ = ((t_) == 0 || (t_) == 16) ? (voffV & ~2048u) : voffV, so_ = hb + ((t_) == 0 ? 0u : (unsigned)(t_) * 4096u - 2048u); \
        _Pragma("unroll") for (int j_ = 0; j_ < 8; ++j_) blds16c(rV, lds + ((t_) & 1) * UNIT + VOFF + ldsw + j_ * 4096, vo_, so_ + j_ * 65536); } while (0)
#define SYNC_S(t_) do { wait_v<0>(); WAIT_L0(); BAR(); asm volatile("" ::: "memory"); if ((t_) + 2 <= qL) STAGE_K((t_) + 2); if ((t_) + 2 <= qL + 1) STAGE_V((t_) + 2); } while (0)
#define SYNC_N(t_, n_) do { wait_v<n_>(); BAR(); asm volatile("" ::: "memory"); if ((t_) + 2 <= qL) STAGE_K((t_) + 2); if ((t_) + 2 <= qL + 1) STAGE_V((t_) + 2); } while (0)
#define SYNC(t_) do { if (grp == 1) SYNC_S(t_); else BAR(); \
        _Pragma("unroll") for (int i_ = 0; i_ < 8; ++i_) c.ka[i_] ^= (unsigned)UNIT; STAMPV(tsv, nst); ++nst; } while (0)
        flag_wait(flags + F_KVA + h * 64 + (lane & 7), tag, true);
        if (grp == 0) { STAGE_K(0); } else { STAGE_V(0); }
#pragma unroll
        for (int d = 0; d < 8; ++d) c.ka[d] = (lbase + (unsigned)(r32 * 256 + ((hi ^ (r32 & 15)) << 4))) ^ (unsigned)(d << 5);
        WAIT_L0(); BAR(); asm volatile("" ::: "memory");
#pragma unroll
        for (int d = 0; d < 8; ++d) c.qr[d] = lds_b128((const LAS unsigned char*)(opaque(c.ka[d] + 98304 + grp * 32768 + wq * 8192)));
        c.dq = 32 * wq + r32 - 4 * hi;
#pragma unroll
        for (int r = 0; r < 16; ++r) { c.o[0][r] = 0.f; c.o[1][r] = 0.f; c.o[2][r] = 0.f; c.o[3][r] = 0.f; c.negm[r] = 0.f; c.s[1][r] = -__builtin_inff(); }
        c.l0 = 0.f; c.l1 = 0.f;
        wait_v<0>();
        WAIT_L0(); BAR(); asm volatile("" ::: "memory");
        if (grp == 1) { flag_wait(flags + F_KV + h * 64 + lane, tag, true); STAGE_K(1); STAGE_V(1); }
        STAMPV(tsv, 14);
        if (grp == 0) __builtin_amdgcn_s_setprio(2);
        int t = 0;
        halfstep<0, true, true>(c, qb == 0 && wq == 0);
        halfstep<1, true, false>(c, qb == 0 && wq <= 1);
        for (; t < qb;) {
            halfstep<2, false, false>(c, false);
            halfstep<3, false, false>(c, false);
            SYNC(t); ++t;
            const bool last = t == qb;
            halfstep<0, false, false>(c, last && wq == 0);
            halfstep<1, false, false>(c, last && wq <= 1);
        }
        if (wq >= 2) {
            halfstep<2, false, false>(c, wq == 2);
            halfstep<3, false, false>(c, true);
            SYNC(t); ++t;
        }
        { const int pos = wq >= 2 ? 0 : 2;
          pv_tail(c, c.p[0], c.ka[0], pos);
          exps<1, 0>(c, ISEQ(16)); fins<1, 0>(c, ISEQ(16));
          pv_tail(c, c.p[1], c.ka[0], pos + 1); }
        { const float rl = 1.0f / swap_add(c.l0 + c.l1);
          const rsrc_t rC = mk_rsrc(ws + WS_CTX, (unsigned)TSZ);
          const unsigned so = (unsigned)(((qb * 4 + wq) * 128 + h * 4) * 2048), vo = (unsigned)(r32 * 64 + 16 * hi);
#pragma unroll
          for (int b = 0; b < 4; ++b)
#pragma unroll
              for (int uu = 0; uu < 2; ++uu) {
                  float v[8];
#pragma unroll
                  for (int j = 0; j < 8; ++j) v[j] = c.o[b][8 * uu + j] * rl;
                  bstore128c((u32x4){cvtpk(v[0], v[1]), cvtpk(v[2], v[3]), cvtpk(v[4], v[5]), cvtpk(v[6], v[7])}, rC, vo, so + 2048 * b + 32 * uu);
              }
        }
        u64* const fctx = flags + F_CTX + (qb * 32 + h) * 4 + wq;
        if (grp == 0) {
            for (; t <= qL; ++t) BAR();
        } else {
            if (t <= qL) { wait_v<8>(); BAR(); asm volatile("" ::: "memory"); if (t + 2 <= qL) STAGE_K(t + 2); if (t + 2 <= qL + 1) STAGE_V(t + 2); ++t; }
            wait_v<0>();
            if (lane == 0) flag_set(fctx, tag);
            STAMPR(tsv, 60);
            unsigned d0;
            { const rsrc_t rWo = mk_rsrc(wo, NQKV * DM * 2); const int e = wq * 64 + lane;
              asm volatile("buffer_load_dword %0, %1, %2, %3 offen" : "=v"(d0) : "v"((unsigned)(e < 144 ? e : 143) * 128u), "s"(rWo), "s"((unsigned)((xcd * 512 + idx * 16) * DM * 2)) : "memory");
              if (t > qL) wait_v<0>(); }
            const int task = (4 - pr) * 32 + h;
            if (pr <= 4 && task < 152) {
                using namespace fb;
                for (; t <= qL - 6; ++t) SYNC_S(t);
                const int j = task & 7, kg = task >> 3, cc = lane & 3, a = lane >> 2, k = 16 * kg + 4 * wq + cc, kk = k < 289 ? k : 288;
                flag_waitN<4>(flags + F_B1, tag, lane, 256);
                if (task == 0 && tid == 256) flag_set(flags + F_EPOCH, tag);
                SYNC_S(t); ++t;
                u16 w_r[16], w_i[16];
                const u16* wrp = wr_ + (8 * a + j) * 289 + kk; const u16* wip = wi_ + (8 * a + j) * 289 + kk;
#pragma unroll
                for (int q = 0; q < 16; ++q) w_r[q] = wrp[q * (128 * 289)];
                SYNC_N(t, 16); ++t;
#pragma unroll
                for (int q = 0; q < 16; ++q) w_i[q] = wip[q * (128 * 289)];
                SYNC_N(t, 16); ++t;
                { const int rr = lane >> 3, ch = lane & 7; const unsigned vo = (unsigned)(rr * KP * 8 + ((((ch >> 1) ^ ((rr >> 1) & 3)) << 1) | (ch & 1)) * 16);
#pragma unroll
                  for (int i = 0; i < 8; ++i) blds16c(rT1, lds + XTRA + (wq * 8 + i) * 1024, vo, (unsigned)(((j * 256 + wq * 64 + i * 8) * KP + kg * 16) * 8)); }
                cf tw[16]; unsigned wpk[16];
#pragma unroll
                for (int p = 1; p < 16; ++p) { tw[p] = twid((float)(a * p) * (1.0f / 256.0f)); PIN(tw[p]); }
#pragma unroll
                for (int q = 0; q < 16; ++q) { wpk[q] = (unsigned)w_r[q] | ((unsigned)w_i[q] << 16); PIN(wpk[q]); }
                SYNC_S(t); ++t;
                LAS unsigned char* L = lds + XTRA + cc * 8;
#define B2SLOT(r) (*(LAS cf*)(L + (r) * 128 + ((wq ^ (((r) >> 1) & 3)) << 5)))
                cf xx[16];
#pragma unroll
                for (int b = 0; b < 16; ++b) xx[b] = B2SLOT(a + 16 * b);
                dft16<-1>(xx);
#pragma unroll
                for (int p = 1; p < 16; ++p) xx[p] = cmulc(xx[p], tw[p]);
#pragma unroll
                for (int p = 0; p < 16; ++p) B2SLOT(16 * p + (a ^ p)) = xx[p];
                SYNC_S(t); ++t;
#pragma unroll
                for (int i = 0; i < 16; ++i) xx[i] = B2SLOT(16 * a + (i ^ a));
                dft16<-1>(xx);
#pragma unroll
                for (int q = 0; q < 16; ++q) xx[q] = cmul(xx[q], (cf){bfl(wpk[q]), bfh(wpk[q])});
                dft16<1>(xx);
#pragma unroll
                for (int i = 1; i < 16; ++i) xx[i] = cmul(xx[i], tw[i]);
#pragma unroll
                for (int i = 0; i < 16; ++i) B2SLOT(16 * a + (i ^ a)) = xx[i];
                SYNC_S(t); ++t;
#pragma unroll
                for (int p = 0; p < 16; ++p) xx[p] = B2SLOT(16 * p + (a ^ p));
                dft16<1>(xx);
#pragma unroll
                for (int b = 0; b < 16; ++b) stc(rT2, (unsigned)(lane * 8), (unsigned)(((j * 76 + kg * 4 + wq) * 256 + 16 * b) * 32), xx[b]);
#undef B2SLOT
                pub = flags + F_B2 + task * 4 + wq;
            }
            for (; t <= qL; ++t) SYNC_S(t);
            asm volatile("" :: "v"(d0));
        }
        if (grp == 0) pub = fctx;
        __builtin_amdgcn_s_setprio(0);
#undef STAGE_K
#undef STAGE_V
#undef SYNC
#undef SYNC_S
#undef SYNC_N
    }
    STAMPV(tsv, 39);
    STAMPV(tsv, 40);
    {
        using namespace k3;
        St s; s.lds = lds;
        const int lane = lane_id(), tid = wid * 64 + lane, wm = wid & 3, nh = wid >> 2, fr = lane & 15, fq = lane >> 4;
        const int sk = xcd >> 1, nt = idx & 3, mt = (pr >> 2) * 8 + (xcd & 1) * 4 + (pr & 3), tile = mt * 4 + nt;
        s.rA = mk_rsrc(ws + WS_CTX, (unsigned)TSZ); s.rB = mk_rsrc(wo, NQKV * DM * 2);
        s.sa = (unsigned)((mt * 4 * 256 + sk * 64) * 1024); s.sb = (unsigned)(sk * 1024 * DM * 2 + nt * 144 * 2);
        s.extra = wid < 2; s.ldsw = wid * 1024;
#pragma unroll
        for (int i = 0; i < 2; ++i) { const int b = tid * 16 + i * 8192, r = b >> 7, kc = ((b >> 4) & 7) ^ ((r >> 1) & 7); s.voffA[i] = (unsigned)(((r >> 5) * 128 + (kc >> 2)) * 2048 + (r & 31) * 64 + (kc & 3) * 16); }
#pragma unroll
        for (int i = 0; i < 3; ++i) { const int b = tid * 16 + i * 8192, rho = (b / 288) & 63, ch = (b % 288) >> 4;
            const int r5 = rho & 31, pos = (rho & 32) + 8 * ((r5 >> 2) & 3) + 4 * (r5 >> 4) + (r5 & 3);
            const int d = (pos & ~15) + 8 * ((pos >> 2) & 1) + 4 * ((pos >> 3) & 1) + (pos & 3);
            s.voffB[i] = (unsigned)(d * DM * 2 + ch * 16); }
        s.aoff[0] = lbase + (unsigned)((wm * 32 + fr) * 128 + ((fq ^ ((fr >> 1) & 7)) * 16)); s.aoff[1] = s.aoff[0] ^ 64u;
        s.boff = lbase + ABYTES + (unsigned)((4 * fq + ((lane & 15) >> 2)) * 288 + 8 * (lane & 3) + nh * 160);
        WAIT_L0(); BAR(); asm volatile("" ::: "memory");
        STAMPV(tsv, 41);
        stageB(s, 0, 0); stageB(s, 1, 1); stageB(s, 2, 2); stageB(s, 3, 3);
        wait_v<8>();
        if (pub != nullptr && lane == 0) flag_set(pub, tag);
        if (wid < 4) STAMPR(tsv, 60);
        if (wid == 7) flag_wait(flags + F_CTX + (mt * 32 + sk * 8) * 4 + (lane & 31), tag, lane < 32);
        BAR(); asm volatile("" ::: "memory");
        STAMPV(tsv, 42); STAMPR(tsv, 61);
        stageA(s, 0, 0); stageA(s, 1, 1); stageA(s, 2, 2); stageA(s, 3, 3);
        wait_v<6>(); BAR();
        STAMPV(tsv, 43);
        if (nh == 0) mainloop<5>(s); else mainloop<4>(s);
        STAMPV(tsv, 44);
        const rsrc_t rS = mk_rsrc(ws + WS_SLAB, 64 * 4 * 4 * 8 * 3 * SLABQ);
        const unsigned sbase = (unsigned)(tile * (4 * 4 * 8 * 3 * SLABQ) + wid * (3 * SLABQ));
        const int x8 = wm, x9 = (wm + 2) & 3;
#pragma unroll
        for (int q = 0; q < 10; ++q) {
            const int own = q < 8 ? (q & 3) : (q == 8 ? x8 : x9), slot = q >> 2;
            if (own != sk && (q < 8 || nh == 0)) bstore128c(__builtin_bit_cast(u32x4, s.acc[q & 1][q >> 1]), rS, lane * 16, sbase + (unsigned)((own * 4 + sk) * (8 * 3 * SLABQ) + slot * SLABQ));
        }
        if (wid == 7) flag_waitN<10>(flags + F_B2, tag, lane, 608);
        BAR(); asm volatile("" ::: "memory");
        fb::cf z[8];
        fb::b3_load(z, rT2, s0, tid);
        wait_v<8>();
        if (lane < 4 && lane != sk) flag_set(flags + F_SLAB + ((tile * 4 + lane) * 8 + wid) * 4 + sk, tag);
        STAMPV(tsv, 45);
        fb::b3(z, out, (LAS fb::cf*)lds, s0, tid);
        STAMPV(tsv, 46);
        flag_wait(flags + F_SLAB + ((tile * 4 + sk) * 8 + wid) * 4 + (lane & 3), tag, (lane & 3) != sk);
        STAMPV(tsv, 47);
        { f32x4 part[3][3];
          const bool has3 = nh == 0 && (x8 == sk || x9 == sk);
#pragma unroll
          for (int j = 0; j < 3; ++j) { const int src = (sk + 1 + j) & 3; const unsigned so = sbase + (unsigned)((sk * 4 + src) * (8 * 3 * SLABQ));
              part[j][0] = __builtin_bit_cast(f32x4, __builtin_amdgcn_raw_buffer_load_b128(rS, lane * 16, so, 16));
              part[j][1] = __builtin_bit_cast(f32x4, __builtin_amdgcn_raw_buffer_load_b128(rS, lane * 16, so + SLABQ, 16));
              part[j][2] = (f32x4){0.f, 0.f, 0.f, 0.f};
              if (has3) part[j][2] = __builtin_bit_cast(f32x4, __builtin_amdgcn_raw_buffer_load_b128(rS, lane * 16, so + 2 * SLABQ, 16)); }
          f32x4 mine[3];
          mine[0] = sk == 0 ? s.acc[0][0] : sk == 1 ? s.acc[1][0] : sk == 2 ? s.acc[0][1] : s.acc[1][1];
          mine[1] = sk == 0 ? s.acc[0][2] : sk == 1 ? s.acc[1][2] : sk == 2 ? s.acc[0][3] : s.acc[1][3];
          mine[2] = x8 == sk ? s.acc[0][4] : s.acc[1][4];
          u16* op = out + (size_t)(mt * 128 + wm * 32 + fr) * (2 * DM) + nt * 144 + nh * 80 + 4 * fq;
#pragma unroll
          for (int i = 0; i < 3; ++i) if (i < 2 || has3) {
              const f32x4 r = (mine[i] + part[0][i]) + (part[1][i] + part[2][i]);
              const int q = i < 2 ? sk + 4 * i : (x8 == sk ? 8 : 9);
              *(u32x2*)(op + (size_t)(q & 1) * 16 * (2 * DM) + 16 * (q >> 1)) = (u32x2){cvtpk(r[0], r[1]), cvtpk(r[2], r[3])};
          } }
    }
}

extern "C" void kernel_launch(void* const* d_in, const int* in_sizes, int n_in, void* d_out, int out_size, void* d_ws, size_t ws_size, hipStream_t stream) {
    (void)in_sizes; (void)n_in; (void)out_size; (void)ws_size;
    (void)hipFuncSetAttribute((const void*)fused, hipFuncAttributeMaxDynamicSharedMemorySize, 163840);
    fused<<<256, 512, 163840, stream>>>((const u16*)d_in[0], (const u16*)d_in[1], (const u16*)d_in[2], (const u16*)d_in[3], (const u16*)d_in[4], (const u16*)d_in[5],
                                        (const u16*)d_in[6], (const u16*)d_in[7], (const u16*)d_in[8], (u16*)d_out, (char*)d_ws);
}
```

```cpp
#include <hip/hip_runtime.h>
#include <stdint.h>
#include <utility>
typedef unsigned short u16;
typedef short bf16x8 __attribute__((ext_vector_type(8)));
typedef short s16x4 __attribute__((ext_vector_type(4)));
typedef float f32x2 __attribute__((ext_vector_type(2)));
typedef float f32x4 __attribute__((ext_vector_type(4)));
typedef float f32x16 __attribute__((ext_vector_type(16)));
typedef unsigned u32x4 __attribute__((ext_vector_type(4)));
typedef unsigned u32x2 __attribute__((ext_vector_type(2)));
typedef __bf16 bf16x2_t __attribute__((ext_vector_type(2)));
#define LAS __attribute__((address_space(3)))
#define DI __device__ __forceinline__
#define BAR() __builtin_amdgcn_s_barrier()
#define SCHED() __builtin_amdgcn_sched_barrier(0)
#define WAIT_L0() asm volatile("s_waitcnt lgkmcnt(0)" ::: "memory")
template <int N> DI void wait_v() { asm volatile("s_waitcnt vmcnt(%0)" ::"n"(N) : "memory"); }
DI unsigned cvtpk(float lo, float hi) { f32x2 v = {lo, hi}; bf16x2_t b = __builtin_convertvector(v, bf16x2_t); return __builtin_bit_cast(unsigned, b); }
DI bf16x8 lds_b128(const LAS unsigned char* p) { return *(const LAS bf16x8*)p; }
template <int OFF> DI s16x4 lds_tr(unsigned a) { s16x4 r; asm volatile("ds_read_b64_tr_b16 %0, %1 offset:%2" : "=v"(r) : "v"(a), "n"(OFF) : "memory"); return r; }
typedef __amdgpu_buffer_rsrc_t rsrc_t;
DI rsrc_t mk_rsrc(const void* p, unsigned bytes) { return __builtin_amdgcn_make_buffer_rsrc((void*)p, 0, bytes, 0x00020000); }
DI void blds16(rsrc_t r, LAS unsigned char* l, unsigned voff, unsigned soff) { __builtin_amdgcn_raw_ptr_buffer_load_lds(r, (LAS void*)l, 16, voff, soff, 0, 0); }
DI void blds16c(rsrc_t r, LAS unsigned char* l, unsigned voff, unsigned soff) { __builtin_amdgcn_raw_ptr_buffer_load_lds(r, (LAS void*)l, 16, voff, soff, 0, 16); }
DI void bstore128c(u32x4 v, rsrc_t r, unsigned voff, unsigned soff) { __builtin_amdgcn_raw_buffer_store_b128(v, r, voff, soff, 16); SCHED(); asm volatile("s_nop 1" ::: "memory"); SCHED(); }
typedef unsigned long long u64;
typedef __attribute__((address_space(1))) u64 gu64;
DI void flag_set(u64* p, u64 tag) { __hip_atomic_store((gu64*)p, tag, __ATOMIC_RELAXED, __HIP_MEMORY_SCOPE_AGENT); }
DI u64 flag_get(const u64* p) { return __hip_atomic_load((gu64*)p, __ATOMIC_RELAXED, __HIP_MEMORY_SCOPE_AGENT); }
DI void flag_wait(const u64* p, u64 tag, bool on) {
    for (unsigned spins = 0; spins < (1u << 20); ++spins) {
        const bool ok = !on || flag_get(p) == tag;
        if (__all(ok)) break;
        __builtin_amdgcn_s_sleep(6);
    }
    asm volatile("" ::: "memory");
}
template <int N> DI void flag_waitN(const u64* p, u64 tag, int lane, int count) {
    for (unsigned spins = 0; spins < (1u << 20); ++spins) {
        u64 v[N]; bool ok = true;
#pragma unroll
        for (int i = 0; i < N; ++i) { const int ix = lane + 64 * i; v[i] = flag_get(p + (ix < count ? ix : 0)); }
#pragma unroll
        for (int i = 0; i < N; ++i) ok &= v[i] == tag;
        if (__all(ok)) break;
        __builtin_amdgcn_s_sleep(6);
    }
    asm volatile("" ::: "memory");
}
template <int N> DI void flag_waitN(const u64* p, const u64* p2, u64 tag, int lane) {
    for (unsigned spins = 0; spins < (1u << 20); ++spins) {
        u64 v[N]; bool ok = true;
#pragma unroll
        for (int i = 0; i < N; ++i) v[i] = flag_get((i < 4 ? p : p2) + lane + 64 * i);
#pragma unroll
        for (int i = 0; i < N; ++i) ok &= v[i] == tag;
        if (__all(ok)) break;
        __builtin_amdgcn_s_sleep(6);
    }
    asm volatile("" ::: "memory");
}
DI unsigned opaque(unsigned x) { asm volatile("" : "+v"(x)); return x; }
DI int lane_id() { int l; asm volatile("v_mbcnt_lo_u32_b32 %0, -1, 0\n\tv_mbcnt_hi_u32_b32 %0, -1, %0" : "=v"(l)); return l; }
template <unsigned C> DI unsigned addc(unsigned x) { unsigned r; asm volatile("v_add_u32 %0, %1, %2" : "=v"(r) : "n"(C), "v"(x)); return r; }

#define STAMPV(tsv, n) do {} while (0)
#define STAMPR(tsv, n) do {} while (0)
#define STAMPOUT(base, tsv) do {} while (0)
constexpr int SEQ = 2048, NH = 32, HD = 128, DM = 576, DC = 512, NQKV = 4096;
constexpr float QSCALE = 0.08838834764831845f * 1.4426950408889634f;

constexpr size_t TSZ = (size_t)SEQ * NQKV * 2;
constexpr size_t WS_FLAG = 0, WS_K = 1 << 20, WS_V = WS_K + TSZ, WS_CTX = WS_V + TSZ, WS_SLAB = WS_CTX + TSZ, WS_T1 = WS_SLAB + (26u << 20), WS_T2 = WS_T1 + (5u << 20), WS_WP = WS_T2 + (5u << 20), WS_STAMP = (size_t)120 << 20;
constexpr int F_EPOCH = 0, F_B1 = 64, F_KVA = F_B1 + 256, F_KV = F_KVA + 32 * 64, F_CTX = F_KV + 32 * 64, F_B2 = F_CTX + 16 * 32 * 4, F_WP = F_B2 + 640, F_SLAB = F_WP + 512;

namespace fb {
typedef f32x2 cf;
constexpr int KP = 304;
constexpr unsigned TBYTES = 8u * 256u * KP * 8u;
DI cf cmul(cf a, cf b) { return (cf){a.x * b.x - a.y * b.y, a.x * b.y + a.y * b.x}; }
DI cf cmulc(cf a, cf b) { return (cf){a.x * b.x + a.y * b.y, a.y * b.x - a.x * b.y}; }
DI cf conj(cf a) { return (cf){a.x, -a.y}; }
template <int S> DI cf muli(cf a) { return S > 0 ? (cf){-a.y, a.x} : (cf){a.y, -a.x}; }
DI cf twid(float rev) { return (cf){__builtin_amdgcn_cosf(rev), __builtin_amdgcn_sinf(rev)}; }
DI cf ldc(rsrc_t r, unsigned voff, unsigned soff) { return __builtin_bit_cast(cf, __builtin_amdgcn_raw_buffer_load_b64(r, voff, soff, 16)); }
DI void stc(rsrc_t r, unsigned voff, unsigned soff, cf v) { __builtin_amdgcn_raw_buffer_store_b64(__builtin_bit_cast(u32x2, v), r, voff, soff, 16); }
template <int S> DI void dft4(cf& a, cf& b, cf& c, cf& d) {
    const cf s0 = a + c, d0 = a - c, s1 = b + d, d1 = muli<S>(b - d);
    a = s0 + s1; c = s0 - s1; b = d0 + d1; d = d0 - d1;
}
template <int S> DI void dft8(cf (&x)[8]) {
    constexpr float R = 0.70710678118654752f;
    dft4<S>(x[0], x[2], x[4], x[6]); dft4<S>(x[1], x[3], x[5], x[7]);
    const cf o1 = (x[3] + muli<S>(x[3])) * R, o2 = muli<S>(x[5]), o3 = (muli<S>(x[7]) - x[7]) * R, o0 = x[1];
    const cf e0 = x[0], e1 = x[2], e2 = x[4], e3 = x[6];
    x[0] = e0 + o0; x[4] = e0 - o0; x[1] = e1 + o1; x[5] = e1 - o1; x[2] = e2 + o2; x[6] = e2 - o2; x[3] = e3 + o3; x[7] = e3 - o3;
}
template <int S> DI void dft3(cf& a, cf& b, cf& c) {
    constexpr float H = 0.86602540378443865f;
    const cf s = b + c, d = muli<S>(b - c) * H, m = a - s * 0.5f;
    a = a + s; b = m + d; c = m - d;
}
template <int S> DI void dft9(cf (&x)[9]) {
    constexpr float C1 = 0.76604444311897801f, S1 = 0.64278760968653933f, C2 = 0.17364817766693035f, S2 = 0.98480775301220806f, C4 = -0.93969262078590838f, S4 = 0.34202014332566873f;
    dft3<S>(x[0], x[3], x[6]); dft3<S>(x[1], x[4], x[7]); dft3<S>(x[2], x[5], x[8]);
    const cf w1 = {C1, S * S1}, w2 = {C2, S * S2}, w4 = {C4, S * S4};
    x[4] = cmul(x[4], w1); x[5] = cmul(x[5], w2); x[7] = cmul(x[7], w2); x[8] = cmul(x[8], w4);
    dft3<S>(x[0], x[1], x[2]); dft3<S>(x[3], x[4], x[5]); dft3<S>(x[6], x[7], x[8]);
    cf t;
    t = x[1]; x[1] = x[3]; x[3] = t; t = x[2]; x[2] = x[6]; x[6] = t; t = x[5]; x[5] = x[7]; x[7] = t;
}
template <int S> DI void dft16(cf (&x)[16]) {
    constexpr float C1 = 0.92387953251128674f, S1 = 0.38268343236508977f, R = 0.70710678118654752f;
#pragma unroll
    for (int n2 = 0; n2 < 4; ++n2) dft4<S>(x[n2], x[4 + n2], x[8 + n2], x[12 + n2]);
    const cf w1 = {C1, S * S1}, w2 = {R, S * R}, w3 = {S1, S * C1}, w6 = {-R, S * R}, w9 = {-C1, -S * S1};
    x[5] = cmul(x[5], w1); x[6] = cmul(x[6], w2); x[7] = cmul(x[7], w3);
    x[9] = cmul(x[9], w2); x[10] = muli<S>(x[10]); x[11] = cmul(x[11], w6);
    x[13] = cmul(x[13], w3); x[14] = cmul(x[14], w6); x[15] = cmul(x[15], w9);
#pragma unroll
    for (int k1 = 0; k1 < 4; ++k1) dft4<S>(x[4 * k1], x[4 * k1 + 1], x[4 * k1 + 2], x[4 * k1 + 3]);
#pragma unroll
    for (int a = 0; a < 4; ++a)
#pragma unroll
        for (int b = a + 1; b < 4; ++b) { const cf t = x[4 * a + b]; x[4 * a + b] = x[4 * b + a]; x[4 * b + a] = t; }
}
constexpr int SEQP = 648;
DI float bfl(unsigned w) { return __uint_as_float(w << 16); }
DI float bfh(unsigned w) { return __uint_as_float(w & 0xffff0000u); }
#define LBAR() do { WAIT_L0(); BAR(); asm volatile("" ::: "memory"); } while (0)
DI int pos3(int p1, int p2, int p3) { return 72 * p1 + 8 * p2 + (p3 ^ (((p2 >> 2) | ((p1 & 1) << 1)) << 1)); }
DI int pos_in(int d) { return pos3(d >> 6, (d >> 3) & 7, d & 7); }
DI int pos_out(int k) { const int k3 = k / 72, r = k - 72 * k3, k2 = r / 9, k1 = r - 9 * k2; return pos3(k1, k2, k3); }
struct Prep { cf t1[9], t2[8], t0[8]; unsigned a1, a2, a3, pa, pb; };
template <bool FWD> DI void prep(Prep& p, LAS cf* L, int s0, int tid) {
    const unsigned base = (unsigned)(uintptr_t)L;
    const int bc = tid & 63, t2 = tid < 288 ? tid : 287, sq = t2 / 72, r = t2 - sq * 72, k1 = r >> 3, c = r & 7;
#pragma unroll
    for (int k = 1; k < 9; ++k) { p.t1[k] = twid((float)(bc * k) * (1.0f / 576.0f)); asm volatile("" : "+v"(p.t1[k])); }
#pragma unroll
    for (int k = 1; k < 8; ++k) { p.t2[k] = twid((float)(c * k) * (1.0f / 64.0f)); p.t0[k] = twid((float)(s0 * k) * (1.0f / 2048.0f)); asm volatile("" : "+v"(p.t2[k]), "+v"(p.t0[k])); }
    p.a1 = base + (unsigned)(((tid >> 6) & 3) * SEQP + pos3(0, bc >> 3, bc & 7)) * 8u;
    p.a2 = base + (unsigned)(sq * SEQP + pos3(k1, 0, c)) * 8u;
    p.a3 = base + (unsigned)(sq * SEQP + pos3(k1, c, 0)) * 8u;
    const int k = tid < 289 ? tid : 288;
    if (FWD) { p.pa = base + (unsigned)pos_out(k) * 8u; p.pb = base + (unsigned)pos_out(k == 0 ? 0 : 576 - k) * 8u; }
    else { p.pa = base + (unsigned)pos_out(2 * t2) * 8u; p.pb = base + (unsigned)pos_out(2 * t2 + 1) * 8u; }
    asm volatile("" : "+v"(p.a1), "+v"(p.a2), "+v"(p.a3), "+v"(p.pa), "+v"(p.pb));
}
template <int S> DI cf twmul(cf a, cf w) { return S > 0 ? cmul(a, w) : cmulc(a, w); }
template <int S, class F> DI void fft576x4(const Prep& pp, int tid, unsigned& g_tsv, int& g_n, F&& hook) {
    if (tid < 256) {
        LAS cf* pe = (LAS cf*)pp.a1; LAS cf* po = (LAS cf*)(pp.a1 ^ 32u); cf x[9];
#pragma unroll
        for (int a = 0; a < 9; ++a) x[a] = ((a & 1) ? po : pe)[72 * a];
        dft9<S>(x);
#pragma unroll
        for (int k = 1; k < 9; ++k) x[k] = twmul<S>(x[k], pp.t1[k]);
#pragma unroll
        for (int a = 0; a < 9; ++a) ((a & 1) ? po : pe)[72 * a] = x[a];
    }
    hook(std::integral_constant<int, 0>{});
    LBAR();
    if (tid < 288) {
        LAS cf* pe = (LAS cf*)pp.a2; LAS cf* po = (LAS cf*)(pp.a2 ^ 16u); cf x[8];
#pragma unroll
        for (int b = 0; b < 8; ++b) x[b] = ((b >> 2) ? po : pe)[8 * b];
        dft8<S>(x);
#pragma unroll
        for (int k = 1; k < 8; ++k) x[k] = twmul<S>(x[k], pp.t2[k]);
#pragma unroll
        for (int b = 0; b < 8; ++b) ((b >> 2) ? po : pe)[8 * b] = x[b];
    }
    hook(std::integral_constant<int, 1>{});
    LBAR();
    if (tid < 288) {
        cf x[8];
#pragma unroll
        for (int c = 0; c < 4; ++c) { const f32x4 v = *(const LAS f32x4*)(pp.a3 ^ (unsigned)(c * 16)); x[2 * c] = (cf){v[0], v[1]}; x[2 * c + 1] = (cf){v[2], v[3]}; }
        dft8<S>(x);
#pragma unroll
        for (int c = 0; c < 4; ++c) *(LAS f32x4*)(pp.a3 ^ (unsigned)(c * 16)) = (f32x4){x[2 * c].x, x[2 * c].y, x[2 * c + 1].x, x[2 * c + 1].y};
    }
    hook(std::integral_constant<int, 2>{});
    LBAR();
}

DI void b1_pre(const unsigned (&w)[8], LAS cf* L, int tid) {
    if (tid < 288) {
        cf v[8];
#pragma unroll
        for (int m = 0; m < 8; ++m) v[m] = (cf){bfl(w[m]), bfh(w[m])};
        constexpr float R = 0.70710678118654752f;
        const cf a0 = v[0] + v[4], a1 = v[0] - v[4], a2 = v[2] + v[6], a3 = v[2] - v[6], b0 = v[1] + v[5], b1_ = v[1] - v[5], b2 = v[3] + v[7], b3 = v[3] - v[7];
        const cf g0 = (a0 + a2) + (b0 + b2), g4 = (a0 + a2) - (b0 + b2);
        const cf g2r = a0 - a2, g2i = b2 - b0;
        const cf p = (b1_ - b3) * R, q = (b1_ + b3) * R;
        const cf g1r = a1 + p, g1i = -(a3 + q), g3r = a1 - p, g3i = a3 - q;
        const int p0 = pos_in(2 * tid);
        *(LAS f32x4*)(L + 0 * SEQP + p0) = (f32x4){g0.x, g4.x, g0.y, g4.y};
        *(LAS f32x4*)(L + 1 * SEQP + p0) = (f32x4){g1r.x, g1i.x, g1r.y, g1i.y};
        *(LAS f32x4*)(L + 2 * SEQP + p0) = (f32x4){g2r.x, g2i.x, g2r.y, g2i.y};
        *(LAS f32x4*)(L + 3 * SEQP + p0) = (f32x4){g3r.x, g3i.x, g3r.y, g3i.y};
    }
}
template <class F> DI void b1_post(const Prep& pp, rsrc_t rT1, int s0, int tid, LAS u64* tagp, u64 tagv, unsigned& g_tsv, int& g_n, F&& hook) {
    LBAR();
    fft576x4<-1>(pp, tid, g_tsv, g_n, hook);
    if (tid < 289) {
        const LAS cf* La = (const LAS cf*)pp.pa; const LAS cf* Lb = (const LAS cf*)pp.pb;
        cf t[8];
        { const cf a = La[0], b = conj(Lb[0]); t[0] = (a + b) * 0.5f; t[4] = muli<-1>(a - b) * 0.5f; }
#pragma unroll
        for (int j = 1; j < 4; ++j) { t[j] = La[j * SEQP]; t[8 - j] = conj(Lb[j * SEQP]); }
        const unsigned so = (unsigned)(s0 * 128), vo = (unsigned)((tid >> 4) * 32768 + (tid & 15) * 8);
        stc(rT1, vo, so, t[0]);
#pragma unroll
        for (int j = 1; j < 8; ++j) stc(rT1, vo, so + j * (19 * 32768), cmulc(t[j], pp.t0[j]));
    }
    if (tid == 448) *tagp = tagv;
    LBAR();
}
DI void b3_load(cf (&z)[8], rsrc_t rT2, int s0, int tid) {
    const int k = tid < 289 ? tid : 288;
#pragma unroll
    for (int j = 0; j < 8; ++j) z[j] = ldc(rT2, (unsigned)((k >> 4) * 32768 + (k & 15) * 8), (unsigned)(s0 * 128 + j * (19 * 32768)));
}
template <class F> DI void b3(const Prep& pp, cf (&z)[8], u16* __restrict__ out, LAS cf* L, int s0, int tid, unsigned& g_tsv, int& g_n, F&& hook) {
    if (tid < 289) {
        const int k = tid;
#pragma unroll
        for (int j = 1; j < 8; ++j) z[j] = cmul(z[j], pp.t0[j]);
        dft8<1>(z);
        const bool edge = (unsigned)(k - 1) >= 287u;
        const int pa = pos_in(k), pb = pos_in(576 - k);
#pragma unroll
        for (int c = 0; c < 4; ++c) {
            cf za = z[2 * c], zb = z[2 * c + 1];
            za.y = edge ? 0.f : za.y; zb.y = edge ? 0.f : zb.y;
            L[c * SEQP + pa] = (cf){za.x - zb.y, za.y + zb.x};
            if (!edge) L[c * SEQP + pb] = (cf){za.x + zb.y, zb.x - za.y};
        }
    }
    LBAR();
    fft576x4<1>(pp, tid, g_tsv, g_n, hook);
    if (tid < 288) {
        constexpr float SC = 1.0f / (2048.0f * 576.0f);
        const LAS cf* La = (const LAS cf*)pp.pa; const LAS cf* Lb = (const LAS cf*)pp.pb;
#pragma unroll
        for (int c = 0; c < 4; ++c) {
            const cf a = La[c * SEQP] * SC, b = Lb[c * SEQP] * SC;
            *(unsigned*)(out + (size_t)(s0 + 256 * (2 * c)) * (2 * DM) + DM + 2 * tid) = cvtpk(a.x, b.x);
            *(unsigned*)(out + (size_t)(s0 + 256 * (2 * c + 1)) * (2 * DM) + DM + 2 * tid) = cvtpk(a.y, b.y);
        }
    }
}
}
namespace k1 {
constexpr int NS = 10, SLOT = 16384, NL = 59, NP = 50, NPRO = 8;
struct HT { int kind, kt, rd; };
constexpr HT ht(int i) {
    if (i < 32) { const int t = i >> 2, e = i & 3;
        return e == 0 ? HT{0, t, 4 * t + 1} : e == 1 ? HT{3, t, 4 * t + 1} : e == 2 ? HT{1, t, 4 * t + 2} : HT{4, t, 4 * t + 3}; }
    const int u = (i - 32) / 3, e = (i - 32) % 3;
    return e == 0 ? HT{2, u, 33 + 2 * u} : e == 1 ? HT{5, u, 33 + 2 * u} : HT{6, u, 34 + 2 * u};
}
constexpr int PKV = 43;
constexpr int PPFE = 12;
constexpr int nstores(int p) { return ((p >= 30 && p <= 45) ? 1 : 0) + (p == PKV ? 1 : 0); }
struct Sched { int issue[NL]; int wait[NP + 1]; int first[NP + 2]; int kvcov; };
constexpr Sched make_sched() {
    Sched s{}; int cnt[NP + 2] = {};
    for (int i = 0; i < NL; ++i) {
        int e = i < NPRO ? 0 : 1;
        if (i >= NS && ht(i - NS).rd + 2 > e) e = ht(i - NS).rd + 2;
        if (i > 0 && s.issue[i - 1] > e) e = s.issue[i - 1];
        while (e > 0 && cnt[e] >= 2) ++e;
        s.issue[i] = e; cnt[e]++;
    }
    int endpos[NL] = {}; int tot = 0, li = 0, prev_last = -1, kvend = 1 << 30; s.kvcov = 1 << 30;
    for (int p = 0; p <= NP; ++p) {
        s.first[p] = li;
        tot += nstores(p);
        if (p == 37) kvend = tot;
        while (li < NL && s.issue[li] == p) { tot += 2; endpos[li] = tot; ++li; }
        int last = -1; for (int i = 0; i < NL; ++i) if (ht(i).rd <= p + 1) last = i;
        if (last > prev_last) { s.wait[p] = (s.issue[last] <= p) ? tot - endpos[last] : -1000; prev_last = last;
            if (s.wait[p] >= 0 && tot - s.wait[p] >= kvend && s.kvcov > p) s.kvcov = p; } else s.wait[p] = -1;
    }
    s.first[NP + 1] = li;
    return s;
}
constexpr Sched SC = make_sched();
constexpr bool sched_ok() { for (int p = 0; p <= NP; ++p) if (SC.wait[p] < -1 || SC.wait[p] > 63) return false; return SC.first[NP + 1] == NL; }
static_assert(sched_ok(), "schedule");
static_assert(SC.kvcov < PKV, "the KV stores must be covered by a wait before the flag");

struct St {
    LAS unsigned char* lds; unsigned tsv;
    rsrc_t rw[3], ra[2]; unsigned sw, sa[3];
    u64 tag; u64 *fkv; int wid, idx; unsigned pf[2], swx;
    unsigned voffW[2], voffC[2], voffX[2];
    unsigned ldsw, aoff, boff[2];
    f32x4 acc[2][2][4][2];
    bf16x8 At[4][2]; s16x4 B0[2][2][2], B1[2][2][2];
    u32x4 pk[16]; rsrc_t rK, rV; unsigned evo, skq, sv;
};
template <int I> DI void stage(St& s) {
    constexpr HT h = ht(I); constexpr int slot = I % NS;
    LAS unsigned char* dst = s.lds + slot * SLOT + s.ldsw;
    if constexpr (h.kind <= 2) { const unsigned so = s.sw + h.kt * (64 * NQKV * 2); blds16(s.rw[h.kind], dst, s.voffW[0], so); blds16(s.rw[h.kind], dst + 8192, s.voffW[1], so); }
    else if constexpr (h.kind <= 4) { const unsigned so = s.sa[0] + (h.kind - 3) * (128 * DC * 2) + h.kt * 128; blds16(s.ra[0], dst, s.voffC[0], so); blds16(s.ra[0], dst + 8192, s.voffC[1], so); }
    else { const unsigned so = s.sa[h.kind - 4] + h.kt * 128; blds16(s.ra[1], dst, s.voffX[0], so); blds16(s.ra[1], dst + 8192, s.voffX[1], so); }
}
template <int I> DI void lda(St& s) {
    constexpr int slot = I % NS;
    const unsigned a0 = addc<slot * SLOT>(s.aoff), a1 = a0 ^ 64u;
#pragma unroll
    for (int m = 0; m < 4; ++m) { s.At[m][0] = lds_b128((const LAS unsigned char*)a0 + m * 2048); s.At[m][1] = lds_b128((const LAS unsigned char*)a1 + m * 2048); }
}
template <int I> DI void ldb(St& s, s16x4 (&B)[2][2][2]) {
    constexpr int slot = I % NS;
#pragma unroll
    for (int n = 0; n < 2; ++n) {
        const unsigned p = addc<slot * SLOT>(s.boff[n]);
        B[n][0][0] = lds_tr<0>(p); B[n][0][1] = lds_tr<1024>(p); B[n][1][0] = lds_tr<8192>(p); B[n][1][1] = lds_tr<9216>(p);
    }
}
template <bool TR, bool ZERO> DI void mma(f32x4 (&acc)[4][2], const bf16x8 (&At)[4][2], const s16x4 (&Bh)[2][2][2]) {
    bf16x8 B[2][2];
#pragma unroll
    for (int n = 0; n < 2; ++n)
#pragma unroll
        for (int k = 0; k < 2; ++k) B[n][k] = __builtin_shufflevector(Bh[n][k][0], Bh[n][k][1], 0, 1, 2, 3, 4, 5, 6, 7);
    __builtin_amdgcn_s_setprio(1);
#pragma unroll
    for (int k = 0; k < 2; ++k)
#pragma unroll
        for (int m = 0; m < 4; ++m)
#pragma unroll
            for (int n = 0; n < 2; ++n) {
                const f32x4 c = (ZERO && k == 0) ? (f32x4){0.f, 0.f, 0.f, 0.f} : acc[m][n];
                acc[m][n] = TR ? __builtin_amdgcn_mfma_f32_16x16x32_bf16(B[n][k], At[m][k], c, 0, 0, 0)
                               : __builtin_amdgcn_mfma_f32_16x16x32_bf16(At[m][k], B[n][k], c, 0, 0, 0);
            }
    __builtin_amdgcn_s_setprio(0);
}
template <bool SCALE> DI void pack_t(const f32x4 (&a)[4][2], u32x4* w) {
#pragma unroll
    for (int m = 0; m < 4; ++m) {
        f32x4 v0 = a[m][0], v1 = a[m][1];
        if (SCALE) { v0 *= QSCALE; v1 *= QSCALE; }
        w[m] = (u32x4){cvtpk(v0[0], v0[1]), cvtpk(v0[2], v0[3]), cvtpk(v1[0], v1[1]), cvtpk(v1[2], v1[3])};
        asm volatile("" : "+v"(w[m]));
    }
}
DI void pack_v(const f32x4 (&a)[4][2], u32x4* w) {
#pragma unroll
    for (int u = 0; u < 2; ++u)
#pragma unroll
        for (int n = 0; n < 2; ++n) {
            const f32x4 v0 = a[2 * u][n], v1 = a[2 * u + 1][n];
            w[2 * u + n] = (u32x4){cvtpk(v0[0], v0[1]), cvtpk(v0[2], v0[3]), cvtpk(v1[0], v1[1]), cvtpk(v1[2], v1[3])};
            asm volatile("" : "+v"(w[2 * u + n]));
        }
}
template <int I> DI void store1(St& s) {
    constexpr int qd = I >> 2, i = I & 3, ai = qd >> 1;
    if constexpr ((qd & 1) == 0) bstore128c(s.pk[I], s.rK, s.evo, s.skq + (ai * 8 + i) * 4096);
    else bstore128c(s.pk[I], s.rV, s.evo, s.sv + (i & 1) * 65536 + (ai * 4 + (i >> 1)) * 1024);
}
DI void mk_voffx(St& s) {
    const int tid = s.wid * 64 + lane_id();
#pragma unroll
    for (int i = 0; i < 2; ++i) { const int b = tid * 16 + i * 8192, r = b >> 7, kc = ((b >> 4) & 7) ^ ((r >> 1) & 7), pr = (r & ~24) | ((r & 8) << 1) | ((r & 16) >> 1);
        s.voffX[i] = (unsigned)(pr * DM * 2 + kc * 16); }
}
DI void warm_q(St& s) {
    const int l = lane_id(), e = (s.wid - 5) * 64 + l, ec = e < 144 ? e : 143;
    s.pf[0] = 0;
    if (s.wid == 3) asm volatile("buffer_load_dword %0, %1, %2, %3 offen" : "=v"(s.pf[0]) : "v"((unsigned)l * 128u), "s"(s.ra[1]), "s"((unsigned)(blockIdx.x * 8 * DM * 2)) : "memory");
    else if (s.wid == 4) asm volatile("buffer_load_dword %0, %1, %2, %3 offen" : "=v"(s.pf[0]) : "v"((unsigned)(64 + (l & 7)) * 128u), "s"(s.ra[1]), "s"((unsigned)(blockIdx.x * 8 * DM * 2)) : "memory");
    else if (s.wid >= 5) asm volatile("buffer_load_dword %0, %1, %2, %3 offen" : "=v"(s.pf[0]) : "v"((unsigned)((ec >> 3) * (NQKV * 2) + (ec & 7) * 128)), "s"(s.rw[2]), "s"(s.swx + (unsigned)(s.idx * 18 * NQKV * 2)) : "memory");
}
template <int A, int B> DI void stage_range(St& s) { if constexpr (A < B) { stage<A>(s); stage_range<A + 1, B>(s); } }
template <int P> DI void phase(St& s) {
    if constexpr (P <= 32) {
        constexpr int t = (P - 1) >> 2, e = (P - 1) & 3;
        if constexpr (e == 0) { ldb<4 * t>(s, s.B0); lda<4 * t + 1>(s); }
        if constexpr (e == 1) ldb<4 * t + 2>(s, s.B1);
        if constexpr (e == 2) lda<4 * t + 3>(s);
    } else {
        constexpr int u = (P - 33) >> 1, e = (P - 33) & 1;
        if constexpr (e == 0) { ldb<32 + 3 * u>(s, s.B0); lda<33 + 3 * u>(s); }
        else lda<34 + 3 * u>(s);
    }
    if constexpr (P == 30) { const int l_ = lane_id(); s.evo = (unsigned)((l_ & 15) * 64 + (l_ >> 4) * 16); pack_t<false>(s.acc[0][0], s.pk); }
    if constexpr (P == 31) pack_v(s.acc[0][1], s.pk + 4);
    if constexpr (P == 32) pack_t<false>(s.acc[1][0], s.pk + 8);
    if constexpr (P == 33) pack_v(s.acc[1][1], s.pk + 12);
    if constexpr (P >= 30 && P <= 45) store1<P - 30>(s);
    if constexpr (P == PKV) { if (lane_id() == 0) flag_set(s.fkv, s.tag); }
    if constexpr (P == PPFE) asm volatile("" :: "v"(s.pf[0]));
    if constexpr (P == SC.issue[33]) mk_voffx(s);
    stage_range<SC.first[P], SC.first[P + 1]>(s);
    if constexpr (SC.wait[P] >= 0) wait_v<SC.wait[P]>();
    BAR(); WAIT_L0(); SCHED();
    if constexpr (P <= 32) {
        constexpr int t = (P - 1) >> 2, e = (P - 1) & 3;
        if constexpr (e == 0) mma<true, t == 0>(s.acc[0][0], s.At, s.B0);
        if constexpr (e == 1) mma<false, t == 0>(s.acc[0][1], s.At, s.B1);
        if constexpr (e == 2) mma<true, t == 0>(s.acc[1][0], s.At, s.B0);
        if constexpr (e == 3) mma<false, t == 0>(s.acc[1][1], s.At, s.B1);
    } else {
        constexpr int u = (P - 33) >> 1, e = (P - 33) & 1;
        mma<true, u == 0>(s.acc[e][0], s.At, s.B0);
    }
    BAR(); SCHED();
    if constexpr (P % 5 == 0) STAMPV(s.tsv, 2 + P / 5);
}
template <int... Ps> DI void phases(St& s, std::integer_sequence<int, Ps...>) { (phase<Ps + 1>(s), ...); }
}

namespace k2 {
constexpr int UNIT = 65536, VOFF = 32768, XTRA = 131072;
constexpr float THR = 8.0f;
#define K2_PD 4
constexpr int PD = K2_PD;
#define MFMA32(a, b, c) __builtin_amdgcn_mfma_f32_32x32x16_bf16((a), (b), (c), 0, 0, 0)
#define PIN(x) asm volatile("" : "+v"(x))
DI float max2f(float a, float b) { return __builtin_elementwise_maximum(a, b); }
DI float max3f(float a, float b, float c) { return max2f(max2f(a, b), c); }
DI float swap_max(float x) { auto r = __builtin_amdgcn_permlane32_swap(__float_as_uint(x), __float_as_uint(x), false, false); return max2f(__uint_as_float(r[0]), __uint_as_float(r[1])); }
DI float swap_add(float x) { auto r = __builtin_amdgcn_permlane32_swap(__float_as_uint(x), __float_as_uint(x), false, false); return __uint_as_float(r[0]) + __uint_as_float(r[1]); }
DI void mask16(f32x16& s, int dq) {
    const float NEG = -__builtin_inff();
#pragma unroll
    for (int r = 0; r < 16; ++r) { const int c = (r & 3) + 8 * (r >> 2); s[r] = c > dq ? NEG : s[r]; }
}
struct St {
    unsigned ka[8];
    bf16x8 qr[8], fr[16 + 8]; u32x4 p[2][2];
    f32x16 o[4], s[2], negm;
    float l0, l1, e[16], mxa, mxb, thr; int dq; unsigned long long bal;
};
template <int I, int POS, bool NOPV> DI void ldfrag(St& c) {
    if constexpr (I < 8) c.fr[I] = lds_b128((const LAS unsigned char*)c.ka[I] + POS * 8192);
    else if constexpr (I < 16) { if constexpr (!NOPV) c.fr[I] = lds_b128((const LAS unsigned char*)c.ka[2 * POS + ((I - 8) >> 2)] + VOFF + ((I - 8) & 3) * 8192); }
    else c.fr[I] = lds_b128((const LAS unsigned char*)c.ka[I - 16] + ((POS + 1) & 3) * 8192);
}
template <int X, int E> DI void do_exp(St& c) { c.e[E] = __builtin_amdgcn_exp2f(c.s[X][E]); PIN(c.e[E]); }
template <int X, int E> DI void do_fin(St& c) {
    if constexpr (E & 1) { c.l1 += c.e[E]; PIN(c.l1); unsigned w = cvtpk(c.e[E - 1], c.e[E]); PIN(w); c.p[X][E >> 3][(E >> 1) & 3] = w; }
    else { c.l0 += c.e[E]; PIN(c.l0); }
}
template <int X, int I> DI void do_max(St& c) {
    const f32x16& v = c.s[X];
    if constexpr (I == 0) c.mxa = max2f(v[0], v[1]);
    else if constexpr (I == 1) c.mxb = max2f(v[2], v[3]);
    else if constexpr (I & 1) c.mxb = max3f(c.mxb, v[2 * I], v[2 * I + 1]);
    else c.mxa = max3f(c.mxa, v[2 * I], v[2 * I + 1]);
    if constexpr (I & 1) PIN(c.mxb); else PIN(c.mxa);
}
template <int G, int POS, bool NOPV> DI void gapA(St& c) {
    constexpr int X = POS & 1;
    c.s[X] = MFMA32(c.fr[G], c.qr[G], G == 0 ? c.negm : c.s[X]);
    if constexpr (POS == 3 && G >= 4) { ldfrag<2 * G, POS, NOPV>(c); ldfrag<2 * G + 1, POS, NOPV>(c); }
    else ldfrag<G + PD, POS, NOPV>(c);
    if constexpr (G >= 1) do_fin<X ^ 1, G - 1>(c);
    do_exp<X ^ 1, G>(c);
    SCHED();
}
template <int G, int POS, bool NOPV> DI void gapB(St& c) {
    constexpr int X = POS & 1, ks = G >> 2, db = G & 3;
    if constexpr (!NOPV) c.o[db] = MFMA32(c.fr[8 + G], __builtin_bit_cast(bf16x8, c.p[X][ks]), c.o[db]);
    if constexpr (POS != 3) ldfrag<8 + G + PD, POS, NOPV>(c);
    if constexpr (G == 0) { do_fin<X ^ 1, 7>(c); do_exp<X ^ 1, 8>(c); do_exp<X ^ 1, 9>(c); }
    if constexpr (G == 1) { do_fin<X ^ 1, 8>(c); do_fin<X ^ 1, 9>(c); do_exp<X ^ 1, 10>(c); }
    if constexpr (G >= 2) do_fin<X ^ 1, 8 + G>(c);
    if constexpr (G >= 2 && G <= 6) do_exp<X ^ 1, 9 + G>(c);
    if constexpr (G >= 2 && G <= 5) { do_max<X, 2 * (G - 2)>(c); do_max<X, 2 * (G - 2) + 1>(c); }
    if constexpr (G == 6) { c.bal = __ballot(max2f(c.mxa, c.mxb) > c.thr); asm volatile("" : "+s"(c.bal)); }
    SCHED();
}
template <int POS, bool NOPV, int O, int... Gs> DI void phaseA(St& c, std::integer_sequence<int, Gs...>) { (gapA<Gs + O, POS, NOPV>(c), ...); }
template <int POS, bool NOPV, int O, int... Gs> DI void phaseB(St& c, std::integer_sequence<int, Gs...>) { (gapB<Gs + O, POS, NOPV>(c), ...); }
template <int POS, int... Is> DI void ldfirst(St& c, std::integer_sequence<int, Is...>) { (ldfrag<Is, POS, false>(c), ...); }
template <int... Is> DI void takefirst(St& c, std::integer_sequence<int, Is...>) { ((c.fr[Is] = c.fr[16 + Is]), ...); }
template <int X, int O, int... Es> DI void exps(St& c, std::integer_sequence<int, Es...>) { (do_exp<X, Es + O>(c), ...); }
template <int X, int O, int... Es> DI void fins(St& c, std::integer_sequence<int, Es...>) { (do_fin<X, Es + O>(c), ...); }
#define ISEQ(n) std::make_integer_sequence<int, n>{}
DI void rescale_o(St& c, float f) {
#pragma unroll
    for (int b = 0; b < 4; ++b)
#pragma unroll
        for (int r = 0; r < 16; ++r) c.o[b][r] *= f;
}
DI float max16(const f32x16& a) {
    float m0 = max3f(a[0], a[1], a[2]), m1 = max3f(a[3], a[4], a[5]);
    m0 = max3f(m0, a[6], a[7]); m1 = max3f(m1, a[8], a[9]); m0 = max3f(m0, a[10], a[11]); m1 = max3f(m1, a[12], a[13]);
    return max3f(m0, m1, max2f(a[14], a[15]));
}
template <int... Is> DI void ldnext(St& c, std::integer_sequence<int, Is...>) { (ldfrag<16 + Is, 3, false>(c), ...); }
template <int POS, bool NOPV, bool FIRST, class F> DI void halfstep(St& c, bool diag, F&& sync) {
    static_assert(PD == 4, "schedule");
    constexpr int X = POS & 1;
    c.thr = diag ? -__builtin_inff() : THR;
    if constexpr (FIRST) { ldfirst<0>(c, ISEQ(PD)); SCHED(); } else takefirst(c, ISEQ(PD));
    phaseA<POS, NOPV, 0>(c, ISEQ(8));
    if constexpr (POS == 3) {
        phaseB<POS, NOPV, 0>(c, ISEQ(2));
        WAIT_L0(); sync();
        ldnext(c, ISEQ(PD)); SCHED();
        phaseB<POS, NOPV, 2>(c, ISEQ(6));
    } else phaseB<POS, NOPV, 0>(c, ISEQ(8));
    asm volatile("" ::: "memory");
    if (FIRST || __builtin_expect(c.bal != 0ull, 0)) {
        mask16(c.s[X], diag ? c.dq - 32 * POS : 64);
        const float mx = swap_max(max16(c.s[X]));
        if (FIRST || __any(mx > THR)) {
            const float dl = FIRST ? mx : max2f(mx, 0.f);
            if constexpr (!FIRST) {
                const float f = __builtin_amdgcn_exp2f(-dl);
                c.l0 *= f; c.l1 *= f; rescale_o(c, f);
#pragma unroll
                for (int i = 0; i < 8; ++i) { const unsigned w = c.p[X ^ 1][i >> 2][i & 3]; c.p[X ^ 1][i >> 2][i & 3] = cvtpk(__uint_as_float(w << 16) * f, __uint_as_float(w & 0xffff0000u) * f); }
            }
#pragma unroll
            for (int r = 0; r < 16; ++r) { c.s[X][r] -= dl; c.negm[r] -= dl; }
        }
    }
}
DI void pv_tail(St& c, const u32x4 (&p)[2], unsigned abase, int pos) {
#pragma unroll
    for (int ks = 0; ks < 2; ++ks) {
        const unsigned a = abase ^ (unsigned)((2 * pos + ks) << 5);
        bf16x8 f[4];
#pragma unroll
        for (int b = 0; b < 4; ++b) f[b] = lds_b128((const LAS unsigned char*)a + VOFF + b * 8192);
#pragma unroll
        for (int b = 0; b < 4; ++b) c.o[b] = MFMA32(f[b], __builtin_bit_cast(bf16x8, p[ks]), c.o[b]);
    }
}
}

namespace k3 {
constexpr int ABYTES = 16384, BBYTES = 18432, STG = ABYTES + BBYTES, NKT = 16, NSTG = 4;
constexpr int SLABQ = 1024;
struct St {
    LAS unsigned char* lds; unsigned tsv; rsrc_t rA, rB; unsigned voffA[2], voffB[3], sa, sb, ldsw; bool extra;
    unsigned aoff[2], boff;
    f32x4 acc[2][5];
    bf16x8 af[2][2]; s16x4 bf[2][5][2];
};
DI void stageA(St& s, int buf, int Tc) {
    LAS unsigned char* d = s.lds + buf * STG + s.ldsw;
    blds16c(s.rA, d, s.voffA[0], s.sa + Tc * 4096); blds16c(s.rA, d + 8192, s.voffA[1], s.sa + Tc * 4096);
}
DI void stageB(St& s, int buf, int Tc) {
    LAS unsigned char* d = s.lds + buf * STG + s.ldsw;
    blds16(s.rB, d + ABYTES, s.voffB[0], s.sb + Tc * (64 * DM * 2)); blds16(s.rB, d + ABYTES + 8192, s.voffB[1], s.sb + Tc * (64 * DM * 2));
    if (s.extra) blds16(s.rB, d + ABYTES + 16384, s.voffB[2], s.sb + Tc * (64 * DM * 2));
}
template <int I> DI void piece(St& s, int buf, int Tc) {
    LAS unsigned char* d = s.lds + buf * STG + s.ldsw;
    if constexpr (I < 2) blds16c(s.rA, d + I * 8192, s.voffA[I], s.sa + Tc * 4096);
    else if constexpr (I < 4) blds16(s.rB, d + ABYTES + (I - 2) * 8192, s.voffB[I - 2], s.sb + Tc * (64 * DM * 2));
    else { if (s.extra) blds16(s.rB, d + ABYTES + 16384, s.voffB[2], s.sb + Tc * (64 * DM * 2)); }
}
template <int I, int NC, int BUF, int KS, int X> DI void ldone(St& s, unsigned a, unsigned b) {
    if constexpr (I < 2) s.af[X][I] = lds_b128((const LAS unsigned char*)a + I * 2048);
    else if constexpr (I < 2 + 2 * NC) { constexpr int c = (I - 2) >> 1, hf = (I - 2) & 1; s.bf[X][c][hf] = lds_tr<c * 32 + hf * 16 * 288 + KS * 32 * 288>(b); }
}
template <int M, int NC, int BUF, int KS, int X, bool ZERO, bool STG_> DI void gap(St& s, unsigned a, unsigned b, int Tn) {
    constexpr int c = M >> 1, m = M & 1;
    const bf16x8 bb = __builtin_shufflevector(s.bf[X][c][0], s.bf[X][c][1], 0, 1, 2, 3, 4, 5, 6, 7);
    s.acc[m][c] = __builtin_amdgcn_mfma_f32_16x16x32_bf16(bb, s.af[X][m], ZERO ? (f32x4){0.f, 0.f, 0.f, 0.f} : s.acc[m][c], 0, 0, 0);
    ldone<2 * M, NC, BUF, KS, X ^ 1>(s, a, b); ldone<2 * M + 1, NC, BUF, KS, X ^ 1>(s, a, b);
    if constexpr (STG_ && (M == 1 || M == 4 || (M == 7 && KS == 0))) piece<KS * 3 + M / 3>(s, (BUF + 3) & 3, Tn);
    SCHED();
}
template <int NC, int BUF, int KS, int X, bool ZERO, bool STG_, int... Ms> DI void kstep(St& s, int Tn, std::integer_sequence<int, Ms...>) {
    const unsigned a = opaque(s.aoff[KS] + BUF * STG), b = s.boff + BUF * STG;
    (gap<Ms, NC, BUF, KS, X, ZERO, STG_>(s, a, b, Tn), ...);
    WAIT_L0(); SCHED();
}
template <int N> DI void waitv2(bool extra) { if (extra) wait_v<N * 5>(); else wait_v<N * 4>(); }
template <int NC, int B0, int W = 2, bool STG_ = true, int FIX = -1> DI void period(St& s, int T) {
    constexpr int B1 = (B0 + 1) & 3;
    if constexpr (FIX >= 0) wait_v<FIX>(); else waitv2<W>(s.extra);
    BAR(); SCHED();
    kstep<NC, B1, 0, 1, false, STG_>(s, T + 4, std::make_integer_sequence<int, 2 * NC>{});
    kstep<NC, B1, 1, 0, false, STG_>(s, T + 4, std::make_integer_sequence<int, 2 * NC>{});
}
template <int NC, int... Is> DI void ldall(St& s, std::integer_sequence<int, Is...>) { const unsigned a = opaque(s.aoff[0]), b = s.boff; (ldone<Is, NC, 0, 0, 0>(s, a, b), ...); }
template <int NC, class F> DI void mainloop(St& s, F&& late) {
    ldall<NC>(s, std::make_integer_sequence<int, 2 + 2 * NC>{});
    WAIT_L0(); SCHED();
    kstep<NC, 0, 1, 0, true, false>(s, 0, std::make_integer_sequence<int, 2 * NC>{});
    period<NC, 0, 2, true, 4>(s, 0); period<NC, 1, 2, true, 6>(s, 1); period<NC, 2>(s, 2); period<NC, 3>(s, 3);
    for (int T = 4; T < 12; T += 4) { period<NC, 0>(s, T); period<NC, 1>(s, T + 1); period<NC, 2>(s, T + 2); period<NC, 3>(s, T + 3); }
    period<NC, 0, 2, false>(s, 12); period<NC, 1, 1, false>(s, 13);
    late(std::integral_constant<int, 0>{});
    wait_v<0>(); BAR(); asm volatile("" ::: "memory");
    late(std::integral_constant<int, 1>{}); SCHED();
    kstep<NC, 3, 0, 1, false, false>(s, 0, std::make_integer_sequence<int, 2 * NC>{});
    kstep<NC, 3, 1, 0, false, false>(s, 0, std::make_integer_sequence<int, 2 * NC>{});
#pragma unroll
    for (int c = 0; c < NC; ++c) {
        const bf16x8 bb = __builtin_shufflevector(s.bf[1][c][0], s.bf[1][c][1], 0, 1, 2, 3, 4, 5, 6, 7);
#pragma unroll
        for (int m = 0; m < 2; ++m) s.acc[m][c] = __builtin_amdgcn_mfma_f32_16x16x32_bf16(bb, s.af[1][m], s.acc[m][c], 0, 0, 0);
    }
}
}

struct Params { const u16 *x, *c, *wq, *wuk, *wuv, *wo, *bx, *wr, *wi; u16* out; char* ws; };

__global__ __launch_bounds__(512, 2) void fused(const u16* __restrict__ x, const u16* __restrict__ cin, const u16* __restrict__ wq_, const u16* __restrict__ wuk, const u16* __restrict__ wuv,
                                                const u16* __restrict__ wo, const u16* __restrict__ bx, const u16* __restrict__ wr_, const u16* __restrict__ wi_, u16* __restrict__ out, char* __restrict__ ws) {
    extern __shared__ __attribute__((aligned(16))) unsigned char smem[];
    LAS unsigned char* lds = (LAS unsigned char*)smem;
    const unsigned lbase = (unsigned)(uintptr_t)lds;
    const int bid = blockIdx.x, xcd = bid & 7, idx = bid >> 3, h = xcd * 4 + (idx & 3), pr = idx >> 2, qL = 15 - pr;
    const int wid = __builtin_amdgcn_readfirstlane(threadIdx.x >> 6);
    unsigned tsv = 0x7f7f7f7f; STAMPV(tsv, 0); STAMPR(tsv, 62);
    const int s0 = xcd * 32 + idx;
    u64* const flags = (u64*)(ws + WS_FLAG);
    const rsrc_t rK = mk_rsrc(ws + WS_K, (unsigned)TSZ), rV = mk_rsrc(ws + WS_V, (unsigned)TSZ);
    const rsrc_t rT1 = mk_rsrc(ws + WS_T1, fb::TBYTES), rT2 = mk_rsrc(ws + WS_T2, fb::TBYTES);
    u64 tag; u64* pub = nullptr;
    k2::St c;
    {
        using namespace k1;
        St s; s.lds = lds; s.tsv = tsv; s.wid = wid; s.idx = idx; s.swx = (unsigned)(xcd * 4 * HD * 2);
        const int tid = threadIdx.x, lane = tid & 63, wr = wid >> 2, wc = wid & 3, fr = lane & 15, fq = lane >> 4;
        u64 ep0 = 0; if (wid == 7) ep0 = flag_get(flags + F_EPOCH);
        unsigned bw[8];
        { const rsrc_t rBx = mk_rsrc(bx, SEQ * DM * 2); const unsigned vo = (unsigned)(tid < 288 ? tid : 287) * 4u;
#pragma unroll
          for (int m = 0; m < 8; ++m) { bw[m] = 0; if (wid < 5) asm volatile("buffer_load_dword %0, %1, %2, %3 offen" : "=v"(bw[m]) : "v"(vo), "s"(rBx), "s"((unsigned)((s0 + 256 * m) * DM * 2)) : "memory"); } }
        s.rw[0] = mk_rsrc(wuk, DC * NQKV * 2); s.rw[1] = mk_rsrc(wuv, DC * NQKV * 2); s.rw[2] = mk_rsrc(wq_, DM * NQKV * 2);
        s.ra[0] = mk_rsrc(cin, SEQ * DC * 2); s.ra[1] = mk_rsrc(x, SEQ * DM * 2);
        s.sw = h * HD * 2; s.sa[0] = pr * 256 * DC * 2; s.sa[1] = qL * 128 * DM * 2; s.sa[2] = pr * 128 * DM * 2;
        s.rK = rK; s.rV = rV;
        s.skq = (unsigned)(((h * 128 + pr * 16 + wr * 4) * 4 + wc) * 1024);
        s.sv = (unsigned)(((h * 8 + 2 * wc) * 64 + pr * 8 + wr * 2) * 1024);
        s.fkv = flags + F_KVA + h * 64 + pr * 8 + wid;
#pragma unroll
        for (int i = 0; i < 2; ++i) {
            const int b = tid * 16 + i * 8192;
            { const int k = b >> 8, u = (b >> 5) & 7, hf = (b >> 4) & 1, f = (k & 3) | (((k >> 3) & 1) << 2); s.voffW[i] = (unsigned)(k * NQKV * 2 + ((u ^ f) * 16 + hf * 8) * 2); }
            { const int r = b >> 7, kc = ((b >> 4) & 7) ^ ((r >> 1) & 7), prr = (r & ~24) | ((r & 8) << 1) | ((r & 16) >> 1);
              s.voffC[i] = (unsigned)(prr * DC * 2 + kc * 16); }
        }
        s.ldsw = wid * 1024;
        stage_range<0, 4>(s);
        SCHED();
        fb::Prep pp; if (wid < 5) fb::prep<true>(pp, (LAS fb::cf*)(lds + 8 * SLOT), s0, tid);
        if (wid < 5) asm volatile("s_waitcnt vmcnt(8)" : "+v"(bw[0]), "+v"(bw[1]), "+v"(bw[2]), "+v"(bw[3]), "+v"(bw[4]), "+v"(bw[5]), "+v"(bw[6]), "+v"(bw[7]) :: "memory");
        fb::b1_pre(bw, (LAS fb::cf*)(lds + 8 * SLOT), tid);
        stage_range<4, NPRO>(s);
        int gn = 50; fb::b1_post(pp, rT1, s0, tid, (LAS u64*)(lds + 8 * SLOT + 24576), ep0, s.tsv, gn,
                                 [&](auto ic) { constexpr int I = decltype(ic)::value; if constexpr (I == 0) warm_q(s); });
        { const u64 ep = *(const LAS u64*)(lds + 8 * SLOT + 24576);
          tag = ((u64)__builtin_amdgcn_readfirstlane((unsigned)(ep >> 32)) << 32) | (u64)__builtin_amdgcn_readfirstlane((unsigned)ep); tag += 1ull; s.tag = tag; }
        s.aoff = lbase + (unsigned)((wr * 64 + fr) * 128 + ((fq ^ ((fr >> 1) & 7)) * 16));
        { const int q = (lane & 15) >> 2, p = lane & 3, f = q | ((fq & 1) << 2);
#pragma unroll
          for (int n = 0; n < 2; ++n) s.boff[n] = lbase + (unsigned)(256 * (8 * fq + q) + 32 * ((2 * wc + n) ^ f) + 8 * p); }
        wait_v<SC.wait[0]>();
        BAR();
        if (wr == 1) BAR();
        STAMPV(s.tsv, 1);
        phases(s, std::make_integer_sequence<int, NP>{});
        if (lane_id() == 0) flag_set(flags + F_KV + h * 64 + pr * 8 + wid, tag);
        if (wr == 0) BAR();
        if (wid == 0 && lane_id() == 0) flag_set(flags + F_B1 + bid, tag);
        pack_t<true>(s.acc[0][0], s.pk); pack_t<true>(s.acc[1][0], s.pk + 4);
        { const int l_ = lane_id(), fr_ = l_ & 15, fq_ = l_ >> 4;
#pragma unroll
          for (int i = 0; i < 8; ++i) { const int m = i & 3, row = wr * 64 + 32 * (m >> 1) + 16 * (fr_ >> 3) + 8 * (m & 1) + (fr_ & 7);
              *(LAS u32x4*)(lds + 98304 + (i >> 2) * 32768 + row * 256 + (((wc * 4 + fq_) ^ (row & 15)) << 4)) = s.pk[i]; } }
        tsv = s.tsv;
    }
    STAMPV(tsv, 13); STAMPR(tsv, 59);
    {
        using namespace k2;
        const int lane = lane_id(), tid = wid * 64 + lane, wq = wid & 3, grp = wid >> 2, r32 = lane & 31, hi = lane >> 5;
        const int qb = grp == 0 ? qL : pr;
        const unsigned hb = (unsigned)h * (SEQ * HD * 2);
        const int a0 = wq, srow = 4 * a0 + (lane >> 4), sch = (lane & 15) ^ srow;
        const unsigned voffK = (unsigned)((sch >> 2) * 1024 + (4 * (a0 & 1) + (lane >> 4)) * 64 + (sch & 3) * 16 + (a0 >> 1) * 4096);
        const unsigned voffV = (unsigned)((sch >> 2) * 1024 + srow * 64 + (sch & 3) * 16);
        const unsigned ldsw = (unsigned)(4 * a0 * 256);
        int nst = 16;
#define STAGE_K(t_) do { _Pragma("unroll") for (int j_ = 0; j_ < 8; ++j_) blds16c(rK, lds + ((t_) & 1) * UNIT + ldsw + j_ * 4096, voffK, hb + (unsigned)(t_) * 32768u + (j_ >> 1) * 8192 + (j_ & 1) * 512); } while (0)
#define STAGE_V(t_) do { const unsigned vo_ = ((t_) == 0 || (t_) == 16) ? (voffV & ~2048u) : voffV, so_ = hb + ((t_) == 0 ? 0u : (unsigned)(t_) * 4096u - 2048u); \
        _Pragma("unroll") for (int j_ = 0; j_ < 8; ++j_) blds16c(rV, lds + ((t_) & 1) * UNIT + VOFF + ldsw + j_ * 4096, vo_, so_ + j_ * 65536); } while (0)
#define STAMP2() do {} while (0)
#define SYNC_S(t_) do { wait_v<0>(); WAIT_L0(); BAR(); asm volatile("" ::: "memory"); if ((t_) + 2 <= qL) STAGE_K((t_) + 2); if ((t_) + 2 <= qL + 1) STAGE_V((t_) + 2); } while (0)
#define SYNC_N(t_, n_) do { wait_v<n_>(); BAR(); asm volatile("" ::: "memory"); if ((t_) + 2 <= qL) STAGE_K((t_) + 2); if ((t_) + 2 <= qL + 1) STAGE_V((t_) + 2); } while (0)
#define SYNC(t_) do { if (grp == 1) SYNC_S(t_); else { STAMP2(); BAR(); asm volatile("" ::: "memory"); } \
        _Pragma("unroll") for (int i_ = 0; i_ < 8; ++i_) c.ka[i_] ^= (unsigned)UNIT; if (nst < 29) { STAMPV(tsv, nst); ++nst; } } while (0)
        flag_wait(flags + F_KVA + h * 64 + (lane & 7), tag, true);
        if (grp == 0) { STAGE_K(0); } else { STAGE_V(0); }
#pragma unroll
        for (int d = 0; d < 8; ++d) c.ka[d] = (lbase + (unsigned)(r32 * 256 + ((hi ^ (r32 & 15)) << 4))) ^ (unsigned)(d << 5);
        WAIT_L0(); BAR(); asm volatile("" ::: "memory");
#pragma unroll
        for (int d = 0; d < 8; ++d) c.qr[d] = lds_b128((const LAS unsigned char*)(opaque(c.ka[d] + 98304 + grp * 32768 + wq * 8192)));
        c.dq = 32 * wq + r32 - 4 * hi;
#pragma unroll
        for (int r = 0; r < 16; ++r) { c.o[0][r] = 0.f; c.o[1][r] = 0.f; c.o[2][r] = 0.f; c.o[3][r] = 0.f; c.negm[r] = 0.f; c.s[1][r] = -__builtin_inff(); }
        c.l0 = 0.f; c.l1 = 0.f;
        wait_v<0>();
        WAIT_L0(); BAR(); asm volatile("" ::: "memory");
        if (grp == 1) { flag_wait(flags + F_KV + h * 64 + lane, tag, true); STAGE_K(1); STAGE_V(1); }
        STAMPV(tsv, 14);
        if (grp == 0) __builtin_amdgcn_s_setprio(2);
        int t = 0;
        auto nosync = [] {};
        auto sync = [&] { SYNC(t); ++t; };
        halfstep<0, true, true>(c, qb == 0 && wq == 0, nosync);
        halfstep<1, true, false>(c, qb == 0 && wq <= 1, nosync);
        for (; t < qb;) {
            halfstep<2, false, false>(c, false, nosync);
            halfstep<3, false, false>(c, false, sync);
            const bool last = t == qb;
            halfstep<0, false, false>(c, last && wq == 0, nosync);
            halfstep<1, false, false>(c, last && wq <= 1, nosync);
        }
        if (wq >= 2) {
            halfstep<2, false, false>(c, wq == 2, nosync);
            halfstep<3, false, false>(c, true, sync);
        }
        { const int pos = wq >= 2 ? 0 : 2;
          pv_tail(c, c.p[0], c.ka[0], pos);
          exps<1, 0>(c, ISEQ(16)); fins<1, 0>(c, ISEQ(16));
          pv_tail(c, c.p[1], c.ka[0], pos + 1); }
        { const float rl = 1.0f / swap_add(c.l0 + c.l1);
          const rsrc_t rC = mk_rsrc(ws + WS_CTX, (unsigned)TSZ);
          const unsigned so = (unsigned)(((qb * 4 + wq) * 128 + h * 4) * 2048), vo = (unsigned)((r32 & 15) * 64 + ((r32 >> 4) * 2 + hi) * 16);
#pragma unroll
          for (int b = 0; b < 4; ++b) {
              u32x4 A, B;
#pragma unroll
              for (int i = 0; i < 4; ++i) {
                  const unsigned xw = cvtpk(c.o[b][2 * i] * rl, c.o[b][2 * i + 1] * rl), yw = cvtpk(c.o[b][8 + 2 * i] * rl, c.o[b][9 + 2 * i] * rl);
                  const auto r = __builtin_amdgcn_permlane16_swap(xw, yw, false, false); A[i] = r[0]; B[i] = r[1];
              }
              bstore128c(A, rC, vo, so + 2048 * b); bstore128c(B, rC, vo, so + 2048 * b + 1024);
          }
        }
        u64* const fctx = flags + F_CTX + (qb * 32 + h) * 4 + wq;
        const int task = (6 - pr) * 32 + h; const bool hostwg = pr <= 6 && task < 152, host = hostwg && grp == 1;
        const int cc = lane & 15, a = 4 * wq + (lane >> 4), blk = (task & 7) * 19 + (task >> 3);
        const unsigned tvo = (unsigned)(a * 128 + cc * 8), tso = (unsigned)(blk * 32768);
        const rsrc_t rWP = mk_rsrc(ws + WS_WP, fb::TBYTES / 2);
        fb::cf xx[16], tw[16]; unsigned wpk[16];
        auto b2_load = [&] {
            flag_waitN<12>(flags + F_B1, flags + F_WP - 256, tag, lane);
#pragma unroll
            for (int b = 0; b < 16; ++b) xx[b] = fb::ldc(rT1, tvo, tso + b * 2048);
#pragma unroll
            for (int q = 0; q < 16; ++q) wpk[q] = __builtin_amdgcn_raw_buffer_load_b32(rWP, tvo >> 1, (tso >> 1) + q * 1024, 16);
#pragma unroll
            for (int p = 1; p < 16; ++p) { tw[p] = fb::twid((float)(a * p) * (1.0f / 256.0f)); PIN(tw[p]); }
        };
        __builtin_amdgcn_s_setprio(0);
        if (grp == 0) {
            STAMPV(tsv, 30);
            for (; t <= qL; ++t) BAR();
            STAMPV(tsv, 31);
            if (hostwg) { BAR(); wait_v<0>(); if (lane == 0) flag_set(fctx, tag); STAMPR(tsv, 60); STAMPV(tsv, 32); }
            else pub = fctx;
        } else {
            if (t <= qL) { SYNC_N(t, 8); ++t; }
            wait_v<0>();
            if (lane == 0) flag_set(fctx, tag);
            STAMPR(tsv, 60);
            unsigned d0;
            { const rsrc_t rWo = mk_rsrc(wo, NQKV * DM * 2); const int e = wq * 64 + lane;
              asm volatile("buffer_load_dword %0, %1, %2, %3 offen" : "=v"(d0) : "v"((unsigned)(e < 144 ? e : 143) * 128u), "s"(rWo), "s"((unsigned)((xcd * 512 + idx * 16) * DM * 2)) : "memory");
              if (t > qL) wait_v<0>(); }
            if (pr < 4) {
                const int i = pr * 32 + h, tt = tid - 256, jj = tt >> 5, uu = (tt >> 4) & 1;
                const u16* wrp = wr_ + (16 * i + 8 * uu + jj) * 289; const u16* wip = wi_ + (16 * i + 8 * uu + jj) * 289;
                u16 fr_[19], fi_[19];
#pragma unroll
                for (int g = 0; g < 19; ++g) { const int k = g < 18 ? 16 * g + cc : 288; fr_[g] = wrp[k]; fi_[g] = wip[k]; }
                SYNC_N(t, 38); ++t;
                unsigned pk_[19];
#pragma unroll
                for (int g = 0; g < 19; ++g) { pk_[g] = (unsigned)fr_[g] | ((unsigned)fi_[g] << 16); PIN(pk_[g]); }
#pragma unroll
                for (int g = 0; g < 19; ++g) __builtin_amdgcn_raw_buffer_store_b32(pk_[g], rWP, (unsigned)(jj * (19 * 16384) + uu * 64 + cc * 4), (unsigned)(g * 16384 + i * 128), 16);
                SYNC_N(t, 19); ++t;
                wait_v<0>();
                if (lane == 0) flag_set(flags + F_WP + i * 4 + wq, tag);
            }
            if (host) {
                using namespace fb;
                for (; t < qL - 1; ++t) SYNC_S(t);
                b2_load();
                if (task == 0 && tid == 256) flag_set(flags + F_EPOCH, tag);
                SYNC_N(t, 32); ++t;
                LAS cf* L = (LAS cf*)(lds + XTRA) + cc;
                STAMPV(tsv, 33);
                dft16<-1>(xx);
#pragma unroll
                for (int p = 1; p < 16; ++p) xx[p] = cmulc(xx[p], tw[p]);
#pragma unroll
                for (int p = 0; p < 16; ++p) L[(a * 16 + p) * 16] = xx[p];
                STAMPV(tsv, 34);
                SYNC_S(t); ++t;
                STAMPV(tsv, 35);
#pragma unroll
                for (int i = 0; i < 16; ++i) xx[i] = L[(i * 16 + a) * 16];
                dft16<-1>(xx);
#pragma unroll
                for (int q = 0; q < 16; ++q) xx[q] = cmul(xx[q], (cf){bfl(wpk[q]), bfh(wpk[q])});
                dft16<1>(xx);
#pragma unroll
                for (int i = 1; i < 16; ++i) xx[i] = cmul(xx[i], tw[i]);
#pragma unroll
                for (int i = 0; i < 16; ++i) L[(i * 16 + a) * 16] = xx[i];
                STAMPV(tsv, 36);
                WAIT_L0(); BAR(); asm volatile("" ::: "memory");
                STAMPV(tsv, 37);
#pragma unroll
                for (int p = 0; p < 16; ++p) xx[p] = L[(a * 16 + p) * 16];
                dft16<1>(xx);
#pragma unroll
                for (int b = 0; b < 16; ++b) stc(rT2, tvo, tso + b * 2048, xx[b]);
                pub = flags + F_B2 + task * 4 + wq;
            }
            for (; t <= qL; ++t) SYNC_S(t);
            asm volatile("" :: "v"(d0));
        }
#undef STAGE_K
#undef STAGE_V
#undef SYNC
#undef SYNC_S
#undef SYNC_N
    }
    STAMPV(tsv, 39);
    STAMPV(tsv, 40);
    {
        using namespace k3;
        St s; s.lds = lds;
        const int lane = lane_id(), tid = wid * 64 + lane, wm = wid & 3, nh = wid >> 2, fr = lane & 15, fq = lane >> 4;
        const int sk = xcd >> 1, nt = idx & 3, mt = (pr >> 2) * 8 + (xcd & 1) * 4 + (pr & 3), tile = mt * 4 + nt;
        s.rA = mk_rsrc(ws + WS_CTX, (unsigned)TSZ); s.rB = mk_rsrc(wo, NQKV * DM * 2);
        s.sa = (unsigned)((mt * 4 * 256 + sk * 64) * 1024); s.sb = (unsigned)(sk * 1024 * DM * 2 + nt * 144 * 2);
        s.extra = wid < 2; s.ldsw = wid * 1024;
#pragma unroll
        for (int i = 0; i < 2; ++i) { const int b = tid * 16 + i * 8192, r = b >> 7, kc = ((b >> 4) & 7) ^ ((r >> 1) & 7); s.voffA[i] = (unsigned)(((r >> 5) * 128 + (kc >> 2)) * 2048 + (r & 31) * 64 + (kc & 3) * 16); }
#pragma unroll
        for (int i = 0; i < 3; ++i) { const int b = tid * 16 + i * 8192, rho = (b / 288) & 63, ch = (b % 288) >> 4;
            const int r5 = rho & 31, pos = (rho & 32) + 8 * ((r5 >> 2) & 3) + 4 * (r5 >> 4) + (r5 & 3);
            const int d = (pos & ~15) + 8 * ((pos >> 2) & 1) + 4 * ((pos >> 3) & 1) + (pos & 3);
            s.voffB[i] = (unsigned)(d * DM * 2 + ch * 16); }
        s.aoff[0] = lbase + (unsigned)((wm * 32 + fr) * 128 + ((fq ^ ((fr >> 1) & 7)) * 16)); s.aoff[1] = s.aoff[0] ^ 64u;
        s.boff = lbase + ABYTES + (unsigned)((4 * fq + ((lane & 15) >> 2)) * 288 + 8 * (lane & 3) + nh * 160);
        const u64* const fin = flags + F_CTX + (mt * 32 + sk * 8) * 4 + (lane & 31);
        const bool early = pr < 4;
        if (early && wid == 7) flag_wait(fin, tag, lane < 32);
        WAIT_L0(); BAR(); asm volatile("" ::: "memory");
        STAMPV(tsv, 41);
        stageB(s, 0, 0); stageB(s, 1, 1); stageB(s, 2, 2); stageB(s, 3, 3);
        if (early) { stageA(s, 0, 0); stageA(s, 1, 1); stageA(s, 2, 2); stageA(s, 3, 3); wait_v<16>(); } else wait_v<8>();
        if (pub != nullptr && lane == 0) flag_set(pub, tag);
        if (wid < 4 && pub != nullptr) STAMPR(tsv, 60);
        LAS fb::cf* const L3 = (LAS fb::cf*)(lds + NSTG * STG);
        fb::Prep pp; if (wid < 5) fb::prep<false>(pp, L3, s0, tid);
        if (!early) {
            if (wid == 7) flag_wait(fin, tag, lane < 32);
            BAR(); asm volatile("" ::: "memory");
            stageA(s, 0, 0); stageA(s, 1, 1); stageA(s, 2, 2); stageA(s, 3, 3);
        }
        STAMPV(tsv, 42); STAMPR(tsv, 61);
        wait_v<6>(); BAR();
        STAMPV(tsv, 43);
        fb::cf z[8];
        auto late = [&](auto ic) {
            constexpr int I = decltype(ic)::value;
            if constexpr (I == 0) { if (wid == 7) flag_waitN<10>(flags + F_B2, tag, lane, 608); }
            else fb::b3_load(z, rT2, s0, tid);
        };
        if (nh == 0) mainloop<5>(s, late); else mainloop<4>(s, late);
        STAMPV(tsv, 44);
        const rsrc_t rS = mk_rsrc(ws + WS_SLAB, 64 * 4 * 4 * 8 * 3 * SLABQ);
        const unsigned sbase = (unsigned)(tile * (4 * 4 * 8 * 3 * SLABQ) + wid * (3 * SLABQ));
        const int x8 = wm, x9 = (wm + 2) & 3;
#pragma unroll
        for (int q = 0; q < 10; ++q) {
            const int own = q < 8 ? (q & 3) : (q == 8 ? x8 : x9), slot = q >> 2;
            if (own != sk && (q < 8 || nh == 0)) bstore128c(__builtin_bit_cast(u32x4, s.acc[q & 1][q >> 1]), rS, lane * 16, sbase + (unsigned)((own * 4 + sk) * (8 * 3 * SLABQ) + slot * SLABQ));
        }
        STAMPV(tsv, 45);
        int gn = 55;
        fb::b3(pp, z, out, L3, s0, tid, tsv, gn, [&](auto ic) {
            if constexpr (decltype(ic)::value == 0) { wait_v<0>(); if (lane < 4 && lane != sk) flag_set(flags + F_SLAB + ((tile * 4 + lane) * 8 + wid) * 4 + sk, tag); } });
        STAMPV(tsv, 46);
        flag_wait(flags + F_SLAB + ((tile * 4 + sk) * 8 + wid) * 4 + (lane & 3), tag, (lane & 3) != sk);
        STAMPV(tsv, 47);
        { f32x4 part[3][3];
          const bool has3 = nh == 0 && (x8 == sk || x9 == sk);
#pragma unroll
          for (int j = 0; j < 3; ++j) { const int src = (sk + 1 + j) & 3; const unsigned so = sbase + (unsigned)((sk * 4 + src) * (8 * 3 * SLABQ));
              part[j][0] = __builtin_bit_cast(f32x4, __builtin_amdgcn_raw_buffer_load_b128(rS, lane * 16, so, 16));
              part[j][1] = __builtin_bit_cast(f32x4, __builtin_amdgcn_raw_buffer_load_b128(rS, lane * 16, so + SLABQ, 16));
              part[j][2] = (f32x4){0.f, 0.f, 0.f, 0.f};
              if (has3) part[j][2] = __builtin_bit_cast(f32x4, __builtin_amdgcn_raw_buffer_load_b128(rS, lane * 16, so + 2 * SLABQ, 16)); }
          f32x4 mine[3];
          mine[0] = sk == 0 ? s.acc[0][0] : sk == 1 ? s.acc[1][0] : sk == 2 ? s.acc[0][1] : s.acc[1][1];
          mine[1] = sk == 0 ? s.acc[0][2] : sk == 1 ? s.acc[1][2] : sk == 2 ? s.acc[0][3] : s.acc[1][3];
          mine[2] = x8 == sk ? s.acc[0][4] : s.acc[1][4];
          u16* op = out + (size_t)(mt * 128 + wm * 32 + fr) * (2 * DM) + nt * 144 + nh * 80 + 4 * fq;
#pragma unroll
          for (int i = 0; i < 3; ++i) if (i < 2 || has3) {
              const f32x4 r = (mine[i] + part[0][i]) + (part[1][i] + part[2][i]);
              const int q = i < 2 ? sk + 4 * i : (x8 == sk ? 8 : 9);
              *(u32x2*)(op + (size_t)(q & 1) * 16 * (2 * DM) + 16 * (q >> 1)) = (u32x2){cvtpk(r[0], r[1]), cvtpk(r[2], r[3])};
          } }
    }
}

extern "C" void kernel_launch(void* const* d_in, const int* in_sizes, int n_in, void* d_out, int out_size, void* d_ws, size_t ws_size, hipStream_t stream) {
    (void)in_sizes; (void)n_in; (void)out_size; (void)ws_size;
    (void)hipFuncSetAttribute((const void*)fused, hipFuncAttributeMaxDynamicSharedMemorySize, 163840);
    fused<<<256, 512, 163840, stream>>>((const u16*)d_in[0], (const u16*)d_in[1], (const u16*)d_in[2], (const u16*)d_in[3], (const u16*)d_in[4], (const u16*)d_in[5],
                                        (const u16*)d_in[6], (const u16*)d_in[7], (const u16*)d_in[8], (u16*)d_out, (char*)d_ws);
}
```

```cpp
#include <hip/hip_runtime.h>
#include <stdint.h>
#include <utility>
#pragma clang diagnostic ignored "-Wint-to-pointer-cast"
typedef unsigned short u16;
typedef short bf16x8 __attribute__((ext_vector_type(8)));
typedef short s16x4 __attribute__((ext_vector_type(4)));
typedef float f32x2 __attribute__((ext_vector_type(2)));
typedef float f32x4 __attribute__((ext_vector_type(4)));
typedef float f32x16 __attribute__((ext_vector_type(16)));
typedef unsigned u32x4 __attribute__((ext_vector_type(4)));
typedef unsigned u32x2 __attribute__((ext_vector_type(2)));
typedef __bf16 bf16x2_t __attribute__((ext_vector_type(2)));
#define LAS __attribute__((address_space(3)))
#define DI __device__ __forceinline__
#define BAR() __builtin_amdgcn_s_barrier()
#define SCHED() __builtin_amdgcn_sched_barrier(0)
#define WAIT_L0() asm volatile("s_waitcnt lgkmcnt(0)" ::: "memory")
template <int N> DI void wait_v() { asm volatile("s_waitcnt vmcnt(%0)" ::"n"(N) : "memory"); }
DI unsigned cvtpk(float lo, float hi) { f32x2 v = {lo, hi}; bf16x2_t b = __builtin_convertvector(v, bf16x2_t); return __builtin_bit_cast(unsigned, b); }
DI bf16x8 lds_b128(const LAS unsigned char* p) { return *(const LAS bf16x8*)p; }
template <int OFF> DI s16x4 lds_tr(unsigned a) { s16x4 r; asm volatile("ds_read_b64_tr_b16 %0, %1 offset:%2" : "=v"(r) : "v"(a), "n"(OFF) : "memory"); return r; }
template <int OFF> DI bf16x8 lds_b128a(unsigned a) { bf16x8 r; asm volatile("ds_read_b128 %0, %1 offset:%2" : "=v"(r) : "v"(a), "n"(OFF) : "memory"); return r; }
template <int N> DI void wait_l() { asm volatile("s_waitcnt lgkmcnt(%0)" ::"n"(N) : "memory"); }
typedef __amdgpu_buffer_rsrc_t rsrc_t;
DI rsrc_t mk_rsrc(const void* p, unsigned bytes) { return __builtin_amdgcn_make_buffer_rsrc((void*)p, 0, bytes, 0x00020000); }
DI void blds16(rsrc_t r, LAS unsigned char* l, unsigned voff, unsigned soff) { __builtin_amdgcn_raw_ptr_buffer_load_lds(r, (LAS void*)l, 16, voff, soff, 0, 0); }
DI void blds16c(rsrc_t r, LAS unsigned char* l, unsigned voff, unsigned soff) { __builtin_amdgcn_raw_ptr_buffer_load_lds(r, (LAS void*)l, 16, voff, soff, 0, 16); }
DI void bstore128c(u32x4 v, rsrc_t r, unsigned voff, unsigned soff) { __builtin_amdgcn_raw_buffer_store_b128(v, r, voff, soff, 16); SCHED(); asm volatile("s_nop 1" ::: "memory"); SCHED(); }
typedef unsigned long long u64;
typedef __attribute__((address_space(1))) u64 gu64;
DI void flag_set(u64* p, u64 tag) { __hip_atomic_store((gu64*)p, tag, __ATOMIC_RELAXED, __HIP_MEMORY_SCOPE_AGENT); }
DI u64 flag_get(const u64* p) { return __hip_atomic_load((gu64*)p, __ATOMIC_RELAXED, __HIP_MEMORY_SCOPE_AGENT); }
DI void flag_peek(u64& v, const u64* p) { asm volatile("global_load_dwordx2 %0, %1, off sc1" : "=v"(v) : "v"(p) : "memory"); }
DI void flag_wait(const u64* p, u64 tag, bool on) {
    for (unsigned spins = 0; spins < (1u << 20); ++spins) {
        const bool ok = !on || flag_get(p) == tag;
        if (__all(ok)) break;
        __builtin_amdgcn_s_sleep(6);
    }
    asm volatile("" ::: "memory");
}
template <int N> DI void flag_waitN(const u64* p, u64 tag, int lane, int count) {
    for (unsigned spins = 0; spins < (1u << 20); ++spins) {
        u64 v[N]; bool ok = true;
#pragma unroll
        for (int i = 0; i < N; ++i) { const int ix = lane + 64 * i; v[i] = flag_get(p + (ix < count ? ix : 0)); }
#pragma unroll
        for (int i = 0; i < N; ++i) ok &= v[i] == tag;
        if (__all(ok)) break;
        __builtin_amdgcn_s_sleep(6);
    }
    asm volatile("" ::: "memory");
}
template <int N> DI void flag_waitN(const u64* p, const u64* p2, u64 tag, int lane) {
    for (unsigned spins = 0; spins < (1u << 20); ++spins) {
        u64 v[N]; bool ok = true;
#pragma unroll
        for (int i = 0; i < N; ++i) v[i] = flag_get((i < 4 ? p : p2) + lane + 64 * i);
#pragma unroll
        for (int i = 0; i < N; ++i) ok &= v[i] == tag;
        if (__all(ok)) break;
        __builtin_amdgcn_s_sleep(6);
    }
    asm volatile("" ::: "memory");
}
DI unsigned opaque(unsigned x) { asm volatile("" : "+v"(x)); return x; }
DI int lane_id() { int l; asm volatile("v_mbcnt_lo_u32_b32 %0, -1, 0\n\tv_mbcnt_hi_u32_b32 %0, -1, %0" : "=v"(l)); return l; }
template <unsigned C> DI unsigned addc(unsigned x) { unsigned r; asm volatile("v_add_u32 %0, %1, %2" : "=v"(r) : "n"(C), "v"(x)); return r; }

constexpr int SEQ = 2048, NH = 32, HD = 128, DM = 576, DC = 512, NQKV = 4096;
constexpr float QSCALE = 0.08838834764831845f * 1.4426950408889634f;

constexpr size_t TSZ = (size_t)SEQ * NQKV * 2;
constexpr size_t WS_FLAG = 0, WS_K = 1 << 20, WS_V = WS_K + TSZ, WS_CTX = WS_V + TSZ, WS_SLAB = WS_CTX + TSZ, WS_T1 = WS_SLAB + (26u << 20), WS_T2 = WS_T1 + (5u << 20), WS_WP = WS_T2 + (5u << 20);
constexpr int F_EPOCH = 0, F_B1 = 64, F_KVA = F_B1 + 256, F_KV = F_KVA + 32 * 64, F_CTX = F_KV + 32 * 64, F_B2 = F_CTX + 16 * 32 * 4, F_WP = F_B2 + 640, F_SLAB = F_WP + 512;

namespace fb {
typedef f32x2 cf;
constexpr int KP = 304;
constexpr unsigned TBYTES = 8u * 256u * KP * 8u;
DI cf cmul(cf a, cf b) { return (cf){a.x * b.x - a.y * b.y, a.x * b.y + a.y * b.x}; }
DI cf cmulc(cf a, cf b) { return (cf){a.x * b.x + a.y * b.y, a.y * b.x - a.x * b.y}; }
DI cf conj(cf a) { return (cf){a.x, -a.y}; }
template <int S> DI cf muli(cf a) { return S > 0 ? (cf){-a.y, a.x} : (cf){a.y, -a.x}; }
DI cf twid(float rev) { return (cf){__builtin_amdgcn_cosf(rev), __builtin_amdgcn_sinf(rev)}; }
DI cf ldc(rsrc_t r, unsigned voff, unsigned soff) { return __builtin_bit_cast(cf, __builtin_amdgcn_raw_buffer_load_b64(r, voff, soff, 16)); }
DI void stc(rsrc_t r, unsigned voff, unsigned soff, cf v) { __builtin_amdgcn_raw_buffer_store_b64(__builtin_bit_cast(u32x2, v), r, voff, soff, 16); }
template <int S> DI void dft4(cf& a, cf& b, cf& c, cf& d) {
    const cf s0 = a + c, d0 = a - c, s1 = b + d, d1 = muli<S>(b - d);
    a = s0 + s1; c = s0 - s1; b = d0 + d1; d = d0 - d1;
}
template <int S> DI void dft8(cf (&x)[8]) {
    constexpr float R = 0.70710678118654752f;
    dft4<S>(x[0], x[2], x[4], x[6]); dft4<S>(x[1], x[3], x[5], x[7]);
    const cf o1 = (x[3] + muli<S>(x[3])) * R, o2 = muli<S>(x[5]), o3 = (muli<S>(x[7]) - x[7]) * R, o0 = x[1];
    const cf e0 = x[0], e1 = x[2], e2 = x[4], e3 = x[6];
    x[0] = e0 + o0; x[4] = e0 - o0; x[1] = e1 + o1; x[5] = e1 - o1; x[2] = e2 + o2; x[6] = e2 - o2; x[3] = e3 + o3; x[7] = e3 - o3;
}
template <int S> DI void dft3(cf& a, cf& b, cf& c) {
    constexpr float H = 0.86602540378443865f;
    const cf s = b + c, d = muli<S>(b - c) * H, m = a - s * 0.5f;
    a = a + s; b = m + d; c = m - d;
}
template <int S> DI void dft9(cf (&x)[9]) {
    constexpr float C1 = 0.76604444311897801f, S1 = 0.64278760968653933f, C2 = 0.17364817766693035f, S2 = 0.98480775301220806f, C4 = -0.93969262078590838f, S4 = 0.34202014332566873f;
    dft3<S>(x[0], x[3], x[6]); dft3<S>(x[1], x[4], x[7]); dft3<S>(x[2], x[5], x[8]);
    const cf w1 = {C1, S * S1}, w2 = {C2, S * S2}, w4 = {C4, S * S4};
    x[4] = cmul(x[4], w1); x[5] = cmul(x[5], w2); x[7] = cmul(x[7], w2); x[8] = cmul(x[8], w4);
    dft3<S>(x[0], x[1], x[2]); dft3<S>(x[3], x[4], x[5]); dft3<S>(x[6], x[7], x[8]);
    cf t;
    t = x[1]; x[1] = x[3]; x[3] = t; t = x[2]; x[2] = x[6]; x[6] = t; t = x[5]; x[5] = x[7]; x[7] = t;
}
template <int S> DI void dft16(cf (&x)[16]) {
    constexpr float C1 = 0.92387953251128674f, S1 = 0.38268343236508977f, R = 0.70710678118654752f;
#pragma unroll
    for (int n2 = 0; n2 < 4; ++n2) dft4<S>(x[n2], x[4 + n2], x[8 + n2], x[12 + n2]);
    const cf w1 = {C1, S * S1}, w2 = {R, S * R}, w3 = {S1, S * C1}, w6 = {-R, S * R}, w9 = {-C1, -S * S1};
    x[5] = cmul(x[5], w1); x[6] = cmul(x[6], w2); x[7] = cmul(x[7], w3);
    x[9] = cmul(x[9], w2); x[10] = muli<S>(x[10]); x[11] = cmul(x[11], w6);
    x[13] = cmul(x[13], w3); x[14] = cmul(x[14], w6); x[15] = cmul(x[15], w9);
#pragma unroll
    for (int k1 = 0; k1 < 4; ++k1) dft4<S>(x[4 * k1], x[4 * k1 + 1], x[4 * k1 + 2], x[4 * k1 + 3]);
#pragma unroll
    for (int a = 0; a < 4; ++a)
#pragma unroll
        for (int b = a + 1; b < 4; ++b) { const cf t = x[4 * a + b]; x[4 * a + b] = x[4 * b + a]; x[4 * b + a] = t; }
}
constexpr int SEQP = 648;
DI float bfl(unsigned w) { return __uint_as_float(w << 16); }
DI float bfh(unsigned w) { return __uint_as_float(w & 0xffff0000u); }
#define LBAR() do { WAIT_L0(); BAR(); asm volatile("" ::: "memory"); } while (0)
DI int pos3(int p1, int p2, int p3) { return 72 * p1 + 8 * p2 + (p3 ^ (((p2 >> 2) | ((p1 & 1) << 1)) << 1)); }
DI int pos_in(int d) { return pos3(d >> 6, (d >> 3) & 7, d & 7); }
DI int pos_out(int k) { const int k3 = k / 72, r = k - 72 * k3, k2 = r / 9, k1 = r - 9 * k2; return pos3(k1, k2, k3); }
struct Prep { cf t1[9], t2[8], t0[8]; unsigned a1, a2, a3, pa, pb; };
template <bool FWD> DI void prep(Prep& p, LAS cf* L, int s0, int tid) {
    const unsigned base = (unsigned)(uintptr_t)L;
    const int bc = tid & 63, t2 = tid < 288 ? tid : 287, sq = t2 / 72, r = t2 - sq * 72, k1 = r >> 3, c = r & 7;
#pragma unroll
    for (int k = 1; k < 9; ++k) { p.t1[k] = twid((float)(bc * k) * (1.0f / 576.0f)); asm volatile("" : "+v"(p.t1[k])); }
#pragma unroll
    for (int k = 1; k < 8; ++k) { p.t2[k] = twid((float)(c * k) * (1.0f / 64.0f)); p.t0[k] = twid((float)(s0 * k) * (1.0f / 2048.0f)); asm volatile("" : "+v"(p.t2[k]), "+v"(p.t0[k])); }
    p.a1 = base + (unsigned)(((tid >> 6) & 3) * SEQP + pos3(0, bc >> 3, bc & 7)) * 8u;
    p.a2 = base + (unsigned)(sq * SEQP + pos3(k1, 0, c)) * 8u;
    p.a3 = base + (unsigned)(sq * SEQP + pos3(k1, c, 0)) * 8u;
    const int k = tid < 289 ? tid : 288;
    if (FWD) { p.pa = base + (unsigned)pos_out(k) * 8u; p.pb = base + (unsigned)pos_out(k == 0 ? 0 : 576 - k) * 8u; }
    else { p.pa = base + (unsigned)pos_out(2 * t2) * 8u; p.pb = base + (unsigned)pos_out(2 * t2 + 1) * 8u; }
    asm volatile("" : "+v"(p.a1), "+v"(p.a2), "+v"(p.a3), "+v"(p.pa), "+v"(p.pb));
}
template <int S> DI cf twmul(cf a, cf w) { return S > 0 ? cmul(a, w) : cmulc(a, w); }
template <int S, class F> DI void fft576x4(const Prep& pp, int tid, F&& hook) {
    if (tid < 256) {
        LAS cf* pe = (LAS cf*)pp.a1; LAS cf* po = (LAS cf*)(pp.a1 ^ 32u); cf x[9];
#pragma unroll
        for (int a = 0; a < 9; ++a) x[a] = ((a & 1) ? po : pe)[72 * a];
        dft9<S>(x);
#pragma unroll
        for (int k = 1; k < 9; ++k) x[k] = twmul<S>(x[k], pp.t1[k]);
#pragma unroll
        for (int a = 0; a < 9; ++a) ((a & 1) ? po : pe)[72 * a] = x[a];
    }
    hook(std::integral_constant<int, 0>{});
    LBAR();
    if (tid < 288) {
        LAS cf* pe = (LAS cf*)pp.a2; LAS cf* po = (LAS cf*)(pp.a2 ^ 16u); cf x[8];
#pragma unroll
        for (int b = 0; b < 8; ++b) x[b] = ((b >> 2) ? po : pe)[8 * b];
        dft8<S>(x);
#pragma unroll
        for (int k = 1; k < 8; ++k) x[k] = twmul<S>(x[k], pp.t2[k]);
#pragma unroll
        for (int b = 0; b < 8; ++b) ((b >> 2) ? po : pe)[8 * b] = x[b];
    }
    hook(std::integral_constant<int, 1>{});
    LBAR();
    if (tid < 288) {
        cf x[8];
#pragma unroll
        for (int c = 0; c < 4; ++c) { const f32x4 v = *(const LAS f32x4*)(pp.a3 ^ (unsigned)(c * 16)); x[2 * c] = (cf){v[0], v[1]}; x[2 * c + 1] = (cf){v[2], v[3]}; }
        dft8<S>(x);
#pragma unroll
        for (int c = 0; c < 4; ++c) *(LAS f32x4*)(pp.a3 ^ (unsigned)(c * 16)) = (f32x4){x[2 * c].x, x[2 * c].y, x[2 * c + 1].x, x[2 * c + 1].y};
    }
    hook(std::integral_constant<int, 2>{});
    LBAR();
}

DI void b1_pre(const unsigned (&w)[8], LAS cf* L, int tid) {
    if (tid < 288) {
        cf v[8];
#pragma unroll
        for (int m = 0; m < 8; ++m) v[m] = (cf){bfl(w[m]), bfh(w[m])};
        constexpr float R = 0.70710678118654752f;
        const cf a0 = v[0] + v[4], a1 = v[0] - v[4], a2 = v[2] + v[6], a3 = v[2] - v[6], b0 = v[1] + v[5], b1_ = v[1] - v[5], b2 = v[3] + v[7], b3 = v[3] - v[7];
        const cf g0 = (a0 + a2) + (b0 + b2), g4 = (a0 + a2) - (b0 + b2);
        const cf g2r = a0 - a2, g2i = b2 - b0;
        const cf p = (b1_ - b3) * R, q = (b1_ + b3) * R;
        const cf g1r = a1 + p, g1i = -(a3 + q), g3r = a1 - p, g3i = a3 - q;
        const int p0 = pos_in(2 * tid);
        *(LAS f32x4*)(L + 0 * SEQP + p0) = (f32x4){g0.x, g4.x, g0.y, g4.y};
        *(LAS f32x4*)(L + 1 * SEQP + p0) = (f32x4){g1r.x, g1i.x, g1r.y, g1i.y};
        *(LAS f32x4*)(L + 2 * SEQP + p0) = (f32x4){g2r.x, g2i.x, g2r.y, g2i.y};
        *(LAS f32x4*)(L + 3 * SEQP + p0) = (f32x4){g3r.x, g3i.x, g3r.y, g3i.y};
    }
}
template <class F> DI void b1_post(const Prep& pp, rsrc_t rT1, int s0, int tid, LAS u64* tagp, u64 tagv, F&& hook) {
    LBAR();
    fft576x4<-1>(pp, tid, hook);
    if (tid < 289) {
        const LAS cf* La = (const LAS cf*)pp.pa; const LAS cf* Lb = (const LAS cf*)pp.pb;
        cf t[8];
        { const cf a = La[0], b = conj(Lb[0]); t[0] = (a + b) * 0.5f; t[4] = muli<-1>(a - b) * 0.5f; }
#pragma unroll
        for (int j = 1; j < 4; ++j) { t[j] = La[j * SEQP]; t[8 - j] = conj(Lb[j * SEQP]); }
        const unsigned so = (unsigned)(s0 * 128), vo = (unsigned)((tid >> 4) * 32768 + (tid & 15) * 8);
        stc(rT1, vo, so, t[0]);
#pragma unroll
        for (int j = 1; j < 8; ++j) stc(rT1, vo, so + j * (19 * 32768), cmulc(t[j], pp.t0[j]));
    }
    if (tid == 448) *tagp = tagv;
    LBAR();
}
DI void b3_load(cf (&z)[8], rsrc_t rT2, int s0, int tid) {
    const int k = tid < 289 ? tid : 288;
#pragma unroll
    for (int j = 0; j < 8; ++j) z[j] = ldc(rT2, (unsigned)((k >> 4) * 32768 + (k & 15) * 8), (unsigned)(s0 * 128 + j * (19 * 32768)));
}
template <class F> DI void b3(const Prep& pp, cf (&z)[8], u16* __restrict__ out, LAS cf* L, int s0, int tid, F&& hook) {
    if (tid < 289) {
        const int k = tid;
#pragma unroll
        for (int j = 1; j < 8; ++j) z[j] = cmul(z[j], pp.t0[j]);
        dft8<1>(z);
        const bool edge = (unsigned)(k - 1) >= 287u;
        const int pa = pos_in(k), pb = pos_in(576 - k);
#pragma unroll
        for (int c = 0; c < 4; ++c) {
            cf za = z[2 * c], zb = z[2 * c + 1];
            za.y = edge ? 0.f : za.y; zb.y = edge ? 0.f : zb.y;
            L[c * SEQP + pa] = (cf){za.x - zb.y, za.y + zb.x};
            if (!edge) L[c * SEQP + pb] = (cf){za.x + zb.y, zb.x - za.y};
        }
    }
    LBAR();
    fft576x4<1>(pp, tid, hook);
}
DI void b3_out(const Prep& pp, u16* __restrict__ out, int s0, int tid) {
    if (tid < 288) {
        constexpr float SC = 1.0f / (2048.0f * 576.0f);
        const LAS cf* La = (const LAS cf*)pp.pa; const LAS cf* Lb = (const LAS cf*)pp.pb;
#pragma unroll
        for (int c = 0; c < 4; ++c) {
            const cf a = La[c * SEQP] * SC, b = Lb[c * SEQP] * SC;
            *(unsigned*)(out + (size_t)(s0 + 256 * (2 * c)) * (2 * DM) + DM + 2 * tid) = cvtpk(a.x, b.x);
            *(unsigned*)(out + (size_t)(s0 + 256 * (2 * c + 1)) * (2 * DM) + DM + 2 * tid) = cvtpk(a.y, b.y);
        }
    }
}
}
namespace k1 {
constexpr int NS = 10, SLOT = 16384, NL = 59, NP = 25, NPRO = 8;
struct HT { int kind, kt, rd; };
constexpr HT ht(int i) {
    if (i < 32) { const int t = i >> 2, e = i & 3;
        return e == 0 ? HT{0, t, 2 * t + 1} : e == 1 ? HT{3, t, 2 * t + 1} : e == 2 ? HT{1, t, 2 * t + 1} : HT{4, t, 2 * t + 2}; }
    const int u = (i - 32) / 3, e = (i - 32) % 3;
    return e == 0 ? HT{2, u, 17 + u} : e == 1 ? HT{5, u, 17 + u} : HT{6, u, 17 + u};
}
constexpr int PKV = 23;
constexpr int PPFE = 7;
constexpr int st_first(int p) { return p <= 16 ? 0 : p <= 20 ? 2 * (p - 16) : p <= 24 ? p - 12 : 12; }
constexpr int nstores(int p) { return st_first(p + 1) - st_first(p) + (p == PKV ? 1 : 0); }
struct Sched { int issue[NL]; int wait[NP + 1]; int first[NP + 2]; int mid[NP + 1]; int kvcov; };
constexpr Sched make_sched() {
    Sched s{}; int cnt[NP + 2] = {};
    for (int i = 0; i < NL; ++i) {
        int e = i < NPRO ? 0 : 1;
        if (i >= NS) { const int w = ht(i - NS).rd + (ht(i - NS).kind == 6 ? 2 : 1); if (w > e) e = w; }
        if (i > 0 && s.issue[i - 1] > e) e = s.issue[i - 1];
        while (e > 0 && cnt[e] >= 3) ++e;
        s.issue[i] = e; cnt[e]++;
    }
    int endpos[NL] = {}; int tot = 0, li = 0, prev_last = -1, kvend = 1 << 30; s.kvcov = 1 << 30;
    for (int p = 0; p <= NP; ++p) {
        s.first[p] = li;
        tot += p == PKV ? 1 : 0;
        int n = 0; while (li + n < NL && s.issue[li + n] == p) ++n;
        const int ne = n;
        for (int i = 0; i < ne; ++i) { tot += 2; endpos[li] = tot; ++li; }
        s.mid[p] = li;
        int last = -1; for (int i = 0; i < NL; ++i) if (ht(i).rd <= p + 1) last = i;
        if (last > prev_last) { s.wait[p] = last < li ? tot - endpos[last] : -1000; prev_last = last;
            if (s.wait[p] >= 0 && tot - s.wait[p] >= kvend && s.kvcov > p) s.kvcov = p; } else s.wait[p] = -1;
        tot += st_first(p + 1) - st_first(p);
        if (st_first(p) < 8 && st_first(p + 1) >= 8) kvend = tot;
        for (int i = ne; i < n; ++i) { tot += 2; endpos[li] = tot; ++li; }
    }
    s.first[NP + 1] = li;
    return s;
}
constexpr Sched SC = make_sched();
constexpr bool sched_ok() { for (int p = 0; p <= NP; ++p) if (SC.wait[p] < -1 || SC.wait[p] > 63) return false; return SC.first[NP + 1] == NL; }
static_assert(sched_ok(), "schedule");
static_assert(SC.kvcov < PKV, "the KV stores must be covered by a wait before the flag");

struct St {
    LAS unsigned char* lds;
    rsrc_t rw[3], ra[2]; unsigned sw, sa[3];
    u64 tag; u64 *fkv; int wid; unsigned pf[1]; const char *wx, *ww;
    unsigned voffW[2], voffC[2], voffX[2];
    unsigned ldsw, aoff, boff[2];
    f32x4 acc[2][2][4][2];
    bf16x8 At[4][2], At2[4][2]; s16x4 B0[2][2][2], B1[2][2][2];
    u32x4 pk[16]; rsrc_t rK, rV; unsigned evo, skq, sv;
};
template <int I> DI void stage(St& s) {
    constexpr HT h = ht(I); constexpr int slot = I % NS;
    LAS unsigned char* dst = s.lds + slot * SLOT + s.ldsw;
    if constexpr (h.kind <= 2) { const unsigned so = s.sw + h.kt * (64 * NQKV * 2); blds16(s.rw[h.kind], dst, s.voffW[0], so); blds16(s.rw[h.kind], dst + 8192, s.voffW[1], so); }
    else if constexpr (h.kind <= 4) { const unsigned so = s.sa[0] + (h.kind - 3) * (1024 * DC * 2) + h.kt * 128; blds16(s.ra[0], dst, s.voffC[0], so); blds16(s.ra[0], dst + 8192, s.voffC[1], so); }
    else { const unsigned so = s.sa[h.kind - 4] + h.kt * 128; blds16(s.ra[1], dst, s.voffX[0], so); blds16(s.ra[1], dst + 8192, s.voffX[1], so); }
}
template <int I> DI void lda(St& s, bf16x8 (&At)[4][2]) {
    constexpr int slot = I % NS;
    const unsigned a0 = addc<slot * SLOT>(s.aoff), a1 = a0 ^ 64u;
#pragma unroll
    for (int m = 0; m < 4; ++m) { At[m][0] = lds_b128((const LAS unsigned char*)a0 + m * 2048); At[m][1] = lds_b128((const LAS unsigned char*)a1 + m * 2048); }
}
template <int I> DI void ldb(St& s, s16x4 (&B)[2][2][2]) {
    constexpr int slot = I % NS;
#pragma unroll
    for (int n = 0; n < 2; ++n) {
        const unsigned p = addc<slot * SLOT>(s.boff[n]);
        B[n][0][0] = lds_tr<0>(p); B[n][0][1] = lds_tr<1024>(p); B[n][1][0] = lds_tr<8192>(p); B[n][1][1] = lds_tr<9216>(p);
    }
}
template <bool TR, bool ZERO> DI void mma(f32x4 (&acc)[4][2], const bf16x8 (&At)[4][2], const s16x4 (&Bh)[2][2][2]) {
    bf16x8 B[2][2];
#pragma unroll
    for (int n = 0; n < 2; ++n)
#pragma unroll
        for (int k = 0; k < 2; ++k) B[n][k] = __builtin_shufflevector(Bh[n][k][0], Bh[n][k][1], 0, 1, 2, 3, 4, 5, 6, 7);
    __builtin_amdgcn_s_setprio(1);
#pragma unroll
    for (int k = 0; k < 2; ++k)
#pragma unroll
        for (int m = 0; m < 4; ++m)
#pragma unroll
            for (int n = 0; n < 2; ++n) {
                const f32x4 c = (ZERO && k == 0) ? (f32x4){0.f, 0.f, 0.f, 0.f} : acc[m][n];
                acc[m][n] = TR ? __builtin_amdgcn_mfma_f32_16x16x32_bf16(B[n][k], At[m][k], c, 0, 0, 0)
                               : __builtin_amdgcn_mfma_f32_16x16x32_bf16(At[m][k], B[n][k], c, 0, 0, 0);
            }
    __builtin_amdgcn_s_setprio(0);
}
template <bool ZERO, int I> DI void mma_ld(St& s, f32x4 (&acc)[4][2], const bf16x8 (&At)[4][2], const s16x4 (&Bh)[2][2][2], bf16x8 (&A2)[4][2]) {
    bf16x8 B[2][2];
#pragma unroll
    for (int n = 0; n < 2; ++n)
#pragma unroll
        for (int k = 0; k < 2; ++k) B[n][k] = __builtin_shufflevector(Bh[n][k][0], Bh[n][k][1], 0, 1, 2, 3, 4, 5, 6, 7);
    const unsigned a0 = addc<(I % NS) * SLOT>(s.aoff), a1 = a0 ^ 64u;
    __builtin_amdgcn_s_setprio(1);
#pragma unroll
    for (int k = 0; k < 2; ++k)
#pragma unroll
        for (int m = 0; m < 4; ++m) {
#pragma unroll
            for (int n = 0; n < 2; ++n) acc[m][n] = __builtin_amdgcn_mfma_f32_16x16x32_bf16(B[n][k], At[m][k], (ZERO && k == 0) ? (f32x4){0.f, 0.f, 0.f, 0.f} : acc[m][n], 0, 0, 0);
            A2[m][k] = lds_b128((const LAS unsigned char*)(k ? a1 : a0) + m * 2048);
            SCHED();
        }
    __builtin_amdgcn_s_setprio(0);
}
template <bool SCALE> DI void pack_t(const f32x4 (&a)[4][2], u32x4* w) {
#pragma unroll
    for (int m = 0; m < 4; ++m) {
        f32x4 v0 = a[m][0], v1 = a[m][1];
        if (SCALE) { v0 *= QSCALE; v1 *= QSCALE; }
        w[m] = (u32x4){cvtpk(v0[0], v0[1]), cvtpk(v0[2], v0[3]), cvtpk(v1[0], v1[1]), cvtpk(v1[2], v1[3])};
        asm volatile("" : "+v"(w[m]));
    }
}
DI void pack_v(const f32x4 (&a)[4][2], u32x4* w) {
#pragma unroll
    for (int u = 0; u < 2; ++u)
#pragma unroll
        for (int n = 0; n < 2; ++n) {
            const f32x4 v0 = a[2 * u][n], v1 = a[2 * u + 1][n];
            w[2 * u + n] = (u32x4){cvtpk(v0[0], v0[1]), cvtpk(v0[2], v0[3]), cvtpk(v1[0], v1[1]), cvtpk(v1[2], v1[3])};
            asm volatile("" : "+v"(w[2 * u + n]));
        }
}
template <int I> DI void store1(St& s) {
    constexpr int qd = I >> 2, i = I & 3, ai = qd >> 1;
    if constexpr ((qd & 1) == 0) bstore128c(s.pk[I], s.rK, s.evo, s.skq + (ai * 64 + i) * 4096);
    else bstore128c(s.pk[I], s.rV, s.evo, s.sv + (i & 1) * 65536 + (ai * 32 + (i >> 1)) * 1024);
}
DI void mk_voffx(St& s) {
    const int tid = s.wid * 64 + lane_id();
#pragma unroll
    for (int i = 0; i < 2; ++i) { const int b = tid * 16 + i * 8192, r = b >> 7, kc = ((b >> 4) & 7) ^ ((r >> 1) & 7), pr = (r & ~24) | ((r & 8) << 1) | ((r & 16) >> 1);
        s.voffX[i] = (unsigned)(pr * DM * 2 + kc * 16); }
}
DI void warm_q(St& s) {
    const int l = lane_id(), e = s.wid * 27 + (l < 27 ? l : 26), ew = e - 72;
    const char* p = e < 72 ? s.wx + e * 128 : s.ww + (size_t)(ew >> 3) * (NQKV * 2) + (ew & 7) * 128;
    asm volatile("global_load_dword %0, %1, off" : "=v"(s.pf[0]) : "v"(p) : "memory");
}
template <int A, int B> DI void stage_range(St& s) { if constexpr (A < B) { stage<A>(s); stage_range<A + 1, B>(s); } }
template <int P> DI void phase(St& s) {
    if constexpr (P == 16) { const int l_ = lane_id(); s.evo = (unsigned)((l_ & 15) * 64 + (l_ >> 4) * 16); pack_t<false>(s.acc[0][0], s.pk); pack_v(s.acc[0][1], s.pk + 4); }
    if constexpr (P == 17) { pack_t<false>(s.acc[1][0], s.pk + 8); pack_v(s.acc[1][1], s.pk + 12); }
    if constexpr (P <= 16) {
        constexpr int t = (P - 1) >> 1, e = (P - 1) & 1;
        if constexpr (e == 0) { ldb<4 * t>(s, s.B0); lda<4 * t + 1>(s, s.At); ldb<4 * t + 2>(s, s.B1); }
        else lda<4 * t + 3>(s, s.At);
    } else {
        constexpr int u = P - 17;
        ldb<32 + 3 * u>(s, s.B0); lda<33 + 3 * u>(s, s.At);
    }
    if constexpr (P == PKV) { if (lane_id() == 0) flag_set(s.fkv, s.tag); }
    if constexpr (P == PPFE) asm volatile("" :: "v"(s.pf[0]));
    if constexpr (P == SC.issue[33]) mk_voffx(s);
    stage_range<SC.first[P], SC.mid[P]>(s);
    if constexpr (SC.wait[P] >= 0) wait_v<SC.wait[P]>();
    WAIT_L0(); BAR(); SCHED();
    if constexpr (P <= 16) {
        constexpr int t = (P - 1) >> 1, e = (P - 1) & 1;
        mma<true, t == 0>(s.acc[e][0], s.At, s.B0);
        if constexpr (st_first(P + 1) > st_first(P)) store1<st_first(P)>(s);
        if constexpr (st_first(P + 1) > st_first(P) + 1) store1<st_first(P) + 1>(s);
        stage_range<SC.mid[P], SC.first[P + 1]>(s); SCHED();
        mma<false, t == 0>(s.acc[e][1], s.At, s.B1);
    } else {
        mma_ld<P == 17, 34 + 3 * (P - 17)>(s, s.acc[0][0], s.At, s.B0, s.At2);
        if constexpr (st_first(P + 1) > st_first(P)) store1<st_first(P)>(s);
        if constexpr (st_first(P + 1) > st_first(P) + 1) store1<st_first(P) + 1>(s);
        stage_range<SC.mid[P], SC.first[P + 1]>(s); SCHED();
        mma<true, P == 17>(s.acc[1][0], s.At2, s.B0);
    }
    BAR(); SCHED();
}
template <int... Ps> DI void phases(St& s, std::integer_sequence<int, Ps...>) { (phase<Ps + 1>(s), ...); }
}

namespace k2 {
constexpr int UNIT = 65536, VOFF = 32768, XTRA = 131072;
constexpr int PD = 4;
#define MFMA32(a, b, c) __builtin_amdgcn_mfma_f32_32x32x16_bf16((a), (b), (c), 0, 0, 0)
#define PIN(x) asm volatile("" : "+v"(x))
DI float max2f(float a, float b) { return __builtin_elementwise_maximum(a, b); }
DI float max3f(float a, float b, float c) { return max2f(max2f(a, b), c); }
DI float swap_max(float x) { auto r = __builtin_amdgcn_permlane32_swap(__float_as_uint(x), __float_as_uint(x), false, false); return max2f(__uint_as_float(r[0]), __uint_as_float(r[1])); }
DI float swap_add(float x) { auto r = __builtin_amdgcn_permlane32_swap(__float_as_uint(x), __float_as_uint(x), false, false); return __uint_as_float(r[0]) + __uint_as_float(r[1]); }
DI void mask16(f32x16& s, int dq) {
    const float NEG = -__builtin_inff();
#pragma unroll
    for (int r = 0; r < 16; ++r) { const int c = (r & 3) + 8 * (r >> 2); s[r] = c > dq ? NEG : s[r]; }
}
constexpr float SUMLIM = 4096.0f;
struct St {
    unsigned ka[8];
    bf16x8 qr[8], fr[16 + 8]; u32x4 p[2][2];
    f32x16 o[4], s[2], negm;
    float l, t0, t1, ts, e[16]; float lim; int dq; unsigned long long bal;
};
template <int I, int POS, bool NOPV> DI void ldfrag(St& c) {
    if constexpr (I < 8) c.fr[I] = lds_b128((const LAS unsigned char*)c.ka[I] + POS * 8192);
    else if constexpr (I < 16) { if constexpr (!NOPV) c.fr[I] = lds_b128((const LAS unsigned char*)c.ka[2 * POS + ((I - 8) >> 2)] + VOFF + ((I - 8) & 3) * 8192); }
    else c.fr[I] = lds_b128((const LAS unsigned char*)c.ka[I - 16] + ((POS + 1) & 3) * 8192);
}
template <int I, int POS, bool NOPV> DI void ldpair(St& c) { ldfrag<I + 1, POS, NOPV>(c); SCHED(); ldfrag<I, POS, NOPV>(c); }
template <int X, int E> DI void do_exp(St& c) { c.e[E] = __builtin_amdgcn_exp2f(c.s[X][E]); PIN(c.e[E]); }
template <int X, int E> DI void do_fin(St& c) {
    if constexpr (E == 0) c.t0 = c.e[0];
    else if constexpr (E == 1) c.t1 = c.e[1];
    else if constexpr (E & 1) { c.t1 += c.e[E]; PIN(c.t1); }
    else { c.t0 += c.e[E]; PIN(c.t0); }
    if constexpr (E & 1) { unsigned w = cvtpk(c.e[E - 1], c.e[E]); PIN(w); c.p[X][E >> 3][(E >> 1) & 3] = w; }
}
constexpr int efirst(int g) { return g <= 1 ? g : g <= 3 ? g + 1 : g <= 8 ? g + 2 : g <= 13 ? g + 3 : 16; }
template <int G, int Y> DI void sm_gap(St& c) {
    if constexpr (G >= 1) { constexpr int a = efirst(G - 1), b = efirst(G); if constexpr (a < b) do_fin<Y, a>(c); if constexpr (a + 1 < b) do_fin<Y, a + 1>(c); }
    { constexpr int a = efirst(G), b = efirst(G + 1); if constexpr (a < b) do_exp<Y, a>(c); if constexpr (a + 1 < b) do_exp<Y, a + 1>(c); }
    if constexpr (G == 13) { c.ts = c.t0 + c.t1; PIN(c.ts); }
    if constexpr (G == 14) { c.bal = __ballot(!(c.ts <= c.lim)); asm volatile("" : "+s"(c.bal)); }
    if constexpr (G == 0) { c.l += c.ts; PIN(c.l); }
}
template <int G, int POS, bool NOPV> DI void gapA(St& c) {
    constexpr int X = POS & 1;
    c.s[X] = MFMA32(c.fr[G], c.qr[G], G == 0 ? c.negm : c.s[X]);
    if constexpr (POS == 3 && G >= 4) ldpair<2 * G, POS, NOPV>(c);
    else if constexpr ((G & 1) == 0) ldpair<G + PD, POS, NOPV>(c);
    sm_gap<G, X ^ 1>(c);
    SCHED();
}
template <int G, int POS, bool NOPV> DI void gapB(St& c) {
    constexpr int X = POS & 1, ks = G >> 2, db = G & 3;
    if constexpr (!NOPV) c.o[db] = MFMA32(c.fr[8 + G], __builtin_bit_cast(bf16x8, c.p[X][ks]), c.o[db]);
    if constexpr (POS != 3 && (G & 1) == 0) ldpair<8 + G + PD, POS, NOPV>(c);
    sm_gap<8 + G, X ^ 1>(c);
    SCHED();
}
template <int POS, bool NOPV, int O, int... Gs> DI void phaseA(St& c, std::integer_sequence<int, Gs...>) { (gapA<Gs + O, POS, NOPV>(c), ...); }
template <int POS, bool NOPV, int O, int... Gs> DI void phaseB(St& c, std::integer_sequence<int, Gs...>) { (gapB<Gs + O, POS, NOPV>(c), ...); }
template <int... Is> DI void takefirst(St& c, std::integer_sequence<int, Is...>) { ((c.fr[Is] = c.fr[16 + Is]), ...); }
template <int X, int O, int... Es> DI void exps(St& c, std::integer_sequence<int, Es...>) { (do_exp<X, Es + O>(c), ...); }
template <int X, int O, int... Es> DI void fins(St& c, std::integer_sequence<int, Es...>) { (do_fin<X, Es + O>(c), ...); }
#define ISEQ(n) std::make_integer_sequence<int, n>{}
DI float max16(const f32x16& a) {
    float m0 = max3f(a[0], a[1], a[2]), m1 = max3f(a[3], a[4], a[5]);
    m0 = max3f(m0, a[6], a[7]); m1 = max3f(m1, a[8], a[9]); m0 = max3f(m0, a[10], a[11]); m1 = max3f(m1, a[12], a[13]);
    return max3f(m0, m1, max2f(a[14], a[15]));
}
template <int Y, bool OTHER> DI void renew(St& c) {
    const float dl = max2f(swap_max(max16(c.s[Y])), 0.f), f = __builtin_amdgcn_exp2f(-dl);
    c.l *= f;
#pragma unroll
    for (int b = 0; b < 4; ++b)
#pragma unroll
        for (int r = 0; r < 16; ++r) c.o[b][r] *= f;
    float ts = 0.f;
#pragma unroll
    for (int i = 0; i < 8; ++i) { const float a = __builtin_amdgcn_exp2f(c.s[Y][2 * i] - dl), b = __builtin_amdgcn_exp2f(c.s[Y][2 * i + 1] - dl); ts += a + b; c.p[Y][i >> 2][i & 3] = cvtpk(a, b); }
    c.ts = ts;
    if constexpr (OTHER) {
#pragma unroll
        for (int r = 0; r < 16; ++r) { c.s[Y ^ 1][r] -= dl; c.negm[r] -= dl; } }
}
template <int POS, bool NOPV, bool FIRST, class F> DI void halfstep(St& c, bool diag, F&& sync) {
    static_assert(PD == 4, "schedule");
    constexpr int X = POS & 1;
    c.lim = diag ? -1.0f : SUMLIM;
    if constexpr (FIRST) { ldpair<0, 0, false>(c); ldpair<2, 0, false>(c); SCHED(); } else takefirst(c, ISEQ(PD));
    phaseA<POS, NOPV, 0>(c, ISEQ(8));
    phaseB<POS, NOPV, 0>(c, ISEQ(2));
    if constexpr (POS == 3) {
        WAIT_L0(); sync();
        ldpair<16, 3, false>(c); ldpair<18, 3, false>(c); SCHED();
    }
    phaseB<POS, NOPV, 2>(c, ISEQ(6));
    asm volatile("" ::: "memory");
    if (FIRST || __builtin_expect(c.bal != 0ull, 0)) {
        mask16(c.s[X], diag ? c.dq - 32 * POS : 64);
        if constexpr (FIRST) {
            const float dl = swap_max(max16(c.s[X]));
#pragma unroll
            for (int r = 0; r < 16; ++r) { c.s[X][r] -= dl; c.negm[r] -= dl; }
        } else if (__any(!(c.ts <= SUMLIM))) renew<X ^ 1, true>(c);
    }
}
DI void finish_last(St& c) {
    c.l += c.ts;
    exps<1, 0>(c, ISEQ(16)); fins<1, 0>(c, ISEQ(16));
    c.ts = c.t0 + c.t1;
    if (__builtin_expect(__any(!(c.ts <= SUMLIM)), 0)) renew<1, false>(c);
    c.l += c.ts;
}
DI void pv_tail(St& c, const u32x4 (&p)[2], unsigned abase, int pos) {
#pragma unroll
    for (int ks = 0; ks < 2; ++ks) {
        const unsigned a = abase ^ (unsigned)((2 * pos + ks) << 5);
        bf16x8 f[4];
#pragma unroll
        for (int b = 0; b < 4; ++b) f[b] = lds_b128((const LAS unsigned char*)a + VOFF + b * 8192);
#pragma unroll
        for (int b = 0; b < 4; ++b) c.o[b] = MFMA32(f[b], __builtin_bit_cast(bf16x8, p[ks]), c.o[b]);
    }
}
}

namespace k3 {
constexpr int ABYTES = 16384, BBYTES = 18432, STG = ABYTES + BBYTES, NKT = 16, NSTG = 4;
constexpr int SLABQ = 1024;
struct St {
    LAS unsigned char* lds; rsrc_t rA, rB; unsigned voffA[2], voffB[3], sa, sb, ldsw; bool extra;
    unsigned aoff[2], boff;
    f32x4 acc[2][5];
    bf16x8 af[4][2]; s16x4 bf[4][5][2];
};
DI void stageA(St& s, int buf, int Tc) {
    LAS unsigned char* d = s.lds + buf * STG + s.ldsw;
    blds16c(s.rA, d, s.voffA[0], s.sa + Tc * 4096); blds16c(s.rA, d + 8192, s.voffA[1], s.sa + Tc * 4096);
}
DI void stageB(St& s, int buf, int Tc) {
    LAS unsigned char* d = s.lds + buf * STG + s.ldsw;
    blds16(s.rB, d + ABYTES, s.voffB[0], s.sb + Tc * (64 * DM * 2)); blds16(s.rB, d + ABYTES + 8192, s.voffB[1], s.sb + Tc * (64 * DM * 2));
    if (s.extra) blds16(s.rB, d + ABYTES + 16384, s.voffB[2], s.sb + Tc * (64 * DM * 2));
}
template <int I> DI void piece(St& s, int buf, int Tc) {
    LAS unsigned char* d = s.lds + buf * STG + s.ldsw;
    if constexpr (I < 2) blds16c(s.rA, d + I * 8192, s.voffA[I], s.sa + Tc * 4096);
    else if constexpr (I < 4) blds16(s.rB, d + ABYTES + (I - 2) * 8192, s.voffB[I - 2], s.sb + Tc * (64 * DM * 2));
    else { if (s.extra) blds16(s.rB, d + ABYTES + 16384, s.voffB[2], s.sb + Tc * (64 * DM * 2)); }
}
template <int I, int NC, int KS, int X> DI void ldone(St& s, unsigned a, unsigned b) {
    if constexpr (I < 2) s.af[X][I] = lds_b128a<I * 2048>(a);
    else if constexpr (I < 2 + 2 * NC) { constexpr int c = (I - 2) >> 1, hf = (I - 2) & 1; s.bf[X][c][hf] = lds_tr<c * 32 + hf * 16 * 288 + KS * 32 * 288>(b); }
}
template <int M, int NC, int BUF, int KS, int X, bool ZERO, bool STG_, bool LD> DI void gap(St& s, unsigned a, unsigned b, int Tn) {
    constexpr int c = M >> 1, m = M & 1;
    const bf16x8 bb = __builtin_shufflevector(s.bf[X][c][0], s.bf[X][c][1], 0, 1, 2, 3, 4, 5, 6, 7);
    s.acc[m][c] = __builtin_amdgcn_mfma_f32_16x16x32_bf16(bb, s.af[X][m], ZERO ? (f32x4){0.f, 0.f, 0.f, 0.f} : s.acc[m][c], 0, 0, 0);
    if constexpr (LD) { if constexpr (M < 2) { ldone<2 * M, NC, KS, (X + 2) & 3>(s, a, b); ldone<2 * M + 1, NC, KS, (X + 2) & 3>(s, a, b); } else ldone<M + 2, NC, KS, (X + 2) & 3>(s, a, b); }
    if constexpr (STG_ && (M == 1 || M == 4 || (M == 7 && KS == 0))) piece<KS * 3 + M / 3>(s, (BUF + 2) & 3, Tn);
    SCHED();
}
template <int NC, int BUF, int KS, int X, bool ZERO, bool STG_, bool LD, int LEFT, int... Ms> DI void kstep(St& s, int Tn, std::integer_sequence<int, Ms...>) {
    const unsigned a = addc<BUF * STG>(s.aoff[KS]), b = addc<BUF * STG>(s.boff);
    wait_l<LEFT>(); SCHED();
    (gap<Ms, NC, BUF, KS, X, ZERO, STG_, LD>(s, a, b, Tn), ...);
}
template <int N> DI void waitv2(bool extra) { if (extra) wait_v<N * 5>(); else wait_v<N * 4>(); }
template <int NC, int B, int W = 1, bool STG_ = true, int FIX = -1, bool ZERO = false> DI void period(St& s, int T) {
    if constexpr (FIX >= 0) wait_v<FIX>(); else waitv2<W>(s.extra);
    BAR(); SCHED();
    kstep<NC, (B + 1) & 3, 0, (2 * B) & 3, ZERO, STG_, true, 2 + 2 * NC>(s, T + 3, std::make_integer_sequence<int, 2 * NC>{});
    kstep<NC, (B + 1) & 3, 1, (2 * B + 1) & 3, false, STG_, true, 2 + 2 * NC>(s, T + 3, std::make_integer_sequence<int, 2 * NC>{});
}
template <int NC, int KS, int... Is> DI void ldall(St& s, std::integer_sequence<int, Is...>) { const unsigned a = s.aoff[KS], b = s.boff; (ldone<Is, NC, KS, KS>(s, a, b), ...); }
template <int NC, class F> DI void mainloop(St& s, F&& late) {
    ldall<NC, 0>(s, std::make_integer_sequence<int, 2 + 2 * NC>{});
    ldall<NC, 1>(s, std::make_integer_sequence<int, 2 + 2 * NC>{});
    SCHED();
    period<NC, 0, 1, false, 4, true>(s, 0); period<NC, 1, 1, true, 2>(s, 1); period<NC, 2>(s, 2); period<NC, 3>(s, 3);
    for (int T = 4; T < 12; T += 4) { period<NC, 0>(s, T); period<NC, 1>(s, T + 1); period<NC, 2>(s, T + 2); period<NC, 3>(s, T + 3); }
    period<NC, 0>(s, 12);
    waitv2<1>(s.extra); BAR(); SCHED();
    late(std::integral_constant<int, 0>{}); SCHED();
    kstep<NC, 2, 0, 2, false, false, true, 2 + 2 * NC>(s, 0, std::make_integer_sequence<int, 2 * NC>{});
    kstep<NC, 2, 1, 3, false, false, true, 2 + 2 * NC>(s, 0, std::make_integer_sequence<int, 2 * NC>{});
    wait_v<0>();
    late(std::integral_constant<int, 1>{});
    BAR(); asm volatile("" ::: "memory");
    late(std::integral_constant<int, 2>{}); SCHED();
    kstep<NC, 3, 0, 0, false, false, true, 2 + 2 * NC>(s, 0, std::make_integer_sequence<int, 2 * NC>{});
    kstep<NC, 3, 1, 1, false, false, true, 2 + 2 * NC>(s, 0, std::make_integer_sequence<int, 2 * NC>{});
    kstep<NC, 0, 0, 2, false, false, false, 2 + 2 * NC>(s, 0, std::make_integer_sequence<int, 2 * NC>{});
    kstep<NC, 0, 1, 3, false, false, false, 0>(s, 0, std::make_integer_sequence<int, 2 * NC>{});
}
}

constexpr int CODE_LINES = 18;

__global__ __launch_bounds__(512, 2) void fused(const u16* __restrict__ x, const u16* __restrict__ cin, const u16* __restrict__ wq_, const u16* __restrict__ wuk, const u16* __restrict__ wuv,
                                                const u16* __restrict__ wo, const u16* __restrict__ bx, const u16* __restrict__ wr_, const u16* __restrict__ wi_, u16* __restrict__ out, char* __restrict__ ws) {
    extern __shared__ __attribute__((aligned(16))) unsigned char smem[];
    LAS unsigned char* lds = (LAS unsigned char*)smem;
    const unsigned lbase = (unsigned)(uintptr_t)lds;
    const int bid = blockIdx.x, xcd = bid & 7, idx = bid >> 3, h = xcd * 4 + (idx & 3), pr = idx >> 2, qL = 15 - pr;
    const int wid = __builtin_amdgcn_readfirstlane(threadIdx.x >> 6);
    const int s0 = xcd * 32 + idx;
    u64* const flags = (u64*)(ws + WS_FLAG);
    const rsrc_t rK = mk_rsrc(ws + WS_K, (unsigned)TSZ), rV = mk_rsrc(ws + WS_V, (unsigned)TSZ);
    const rsrc_t rT1 = mk_rsrc(ws + WS_T1, fb::TBYTES), rT2 = mk_rsrc(ws + WS_T2, fb::TBYTES);
    u64 tag; u64* pub = nullptr;
    k2::St c;
    u64 kv0;
    u32x4 dk[4];
    {
        using namespace k1;
        St s; s.lds = lds; s.wid = wid;
        s.wx = (const char*)(x + (size_t)bid * 8 * DM); s.ww = (const char*)(wq_ + (size_t)idx * 18 * NQKV + xcd * 4 * HD);
        const int tid = threadIdx.x, lane = tid & 63, wr = wid >> 2, wc = wid & 3, fr = lane & 15, fq = lane >> 4;
        unsigned cw;
        { const char* pc = (const char*)__builtin_amdgcn_s_getpc(); const int e = wid * 3 + (lane < 3 ? lane : 2), ln = idx * CODE_LINES + (e < CODE_LINES ? e : CODE_LINES - 1);
          asm volatile("global_load_dword %0, %1, off" : "=v"(cw) : "v"(pc + (size_t)ln * 128) : "memory"); }
        unsigned bw[8];
        { const rsrc_t rBx = mk_rsrc(bx, SEQ * DM * 2); const unsigned vo = (unsigned)(tid < 288 ? tid : 287) * 4u;
#pragma unroll
          for (int m = 0; m < 8; ++m) asm volatile("buffer_load_dword %0, %1, %2, %3 offen" : "=v"(bw[m]) : "v"(vo), "s"(rBx), "s"((unsigned)((s0 + 256 * m) * DM * 2)) : "memory"); }
        s.rw[0] = mk_rsrc(wuk, DC * NQKV * 2); s.rw[1] = mk_rsrc(wuv, DC * NQKV * 2); s.rw[2] = mk_rsrc(wq_, DM * NQKV * 2);
        s.ra[0] = mk_rsrc(cin, SEQ * DC * 2); s.ra[1] = mk_rsrc(x, SEQ * DM * 2);
        s.sw = h * HD * 2; s.sa[0] = pr * 128 * DC * 2; s.sa[1] = qL * 128 * DM * 2; s.sa[2] = pr * 128 * DM * 2;
        s.rK = rK; s.rV = rV;
        s.skq = (unsigned)(((h * 128 + pr * 8 + wr * 4) * 4 + wc) * 1024);
        s.sv = (unsigned)(((h * 8 + 2 * wc) * 64 + pr * 4 + wr * 2) * 1024);
        s.fkv = flags + F_KVA + h * 64 + pr * 8 + wid;
#pragma unroll
        for (int i = 0; i < 2; ++i) {
            const int b = tid * 16 + i * 8192;
            { const int k = b >> 8, u = (b >> 5) & 7, hf = (b >> 4) & 1, f = (k & 3) | (((k >> 3) & 1) << 2); s.voffW[i] = (unsigned)(k * NQKV * 2 + ((u ^ f) * 16 + hf * 8) * 2); }
            { const int r = b >> 7, kc = ((b >> 4) & 7) ^ ((r >> 1) & 7), prr = (r & ~24) | ((r & 8) << 1) | ((r & 16) >> 1);
              s.voffC[i] = (unsigned)(prr * DC * 2 + kc * 16); }
        }
        s.ldsw = wid * 1024;
        stage_range<0, 4>(s);
        SCHED();
        fb::Prep pp; if (wid < 5) fb::prep<true>(pp, (LAS fb::cf*)(lds + 8 * SLOT), s0, tid);
        asm volatile("s_waitcnt vmcnt(8)" : "+v"(bw[0]), "+v"(bw[1]), "+v"(bw[2]), "+v"(bw[3]), "+v"(bw[4]), "+v"(bw[5]), "+v"(bw[6]), "+v"(bw[7]) :: "memory");
        fb::b1_pre(bw, (LAS fb::cf*)(lds + 8 * SLOT), tid);
        u64 ep0 = 0; if (wid == 7) ep0 = flag_get(flags + F_EPOCH);
        stage_range<4, NPRO>(s);
        fb::b1_post(pp, rT1, s0, tid, (LAS u64*)(lds + 8 * SLOT + 24576), ep0,
                                 [&](auto ic) { constexpr int I = decltype(ic)::value; if constexpr (I == 0) warm_q(s); });
        { const u64 ep = *(const LAS u64*)(lds + 8 * SLOT + 24576);
          tag = ((u64)__builtin_amdgcn_readfirstlane((unsigned)(ep >> 32)) << 32) | (u64)__builtin_amdgcn_readfirstlane((unsigned)ep); tag += 1ull; s.tag = tag; }
        s.aoff = lbase + (unsigned)((wr * 64 + fr) * 128 + ((fq ^ ((fr >> 1) & 7)) * 16));
        { const int q = (lane & 15) >> 2, p = lane & 3, f = q | ((fq & 1) << 2);
#pragma unroll
          for (int n = 0; n < 2; ++n) s.boff[n] = lbase + (unsigned)(256 * (8 * fq + q) + 32 * ((2 * wc + n) ^ f) + 8 * p); }
        wait_v<SC.wait[0]>();
        asm volatile("" :: "v"(cw));
        BAR();
        if (wr == 1) BAR();
        phases(s, std::make_integer_sequence<int, NP>{});
        flag_peek(kv0, flags + F_KVA + h * 64 + lane_id());
        if (wr == 0) BAR();
        if (wid == 0 && lane_id() == 0) flag_set(flags + F_B1 + bid, tag);
        pack_t<true>(s.acc[0][0], s.pk); pack_t<true>(s.acc[1][0], s.pk + 4);
        { const int l_ = lane_id(), fr_ = l_ & 15, fq_ = l_ >> 4;
#pragma unroll
          for (int i = 0; i < 8; ++i) { const int m = i & 3, row = wr * 64 + 32 * (m >> 1) + 16 * (fr_ >> 3) + 8 * (m & 1) + (fr_ & 7);
              *(LAS u32x4*)(lds + 98304 + (i >> 2) * 32768 + row * 256 + (((wc * 4 + fq_) ^ (row & 15)) << 4)) = s.pk[i]; } }
#pragma unroll
        for (int i = 0; i < 4; ++i) dk[i] = s.pk[12 + i];
    }
    {
        using namespace k2;
        const int lane = lane_id(), tid = wid * 64 + lane, wq = wid & 3, grp = wid >> 2, r32 = lane & 31, hi = lane >> 5;
        const int qb = grp == 0 ? qL : pr;
        const unsigned hb = (unsigned)h * (SEQ * HD * 2);
        const int a0 = wq, srow = 4 * a0 + (lane >> 4), sch = (lane & 15) ^ srow;
        const unsigned voffK = (unsigned)((sch >> 2) * 1024 + (4 * (a0 & 1) + (lane >> 4)) * 64 + (sch & 3) * 16 + (a0 >> 1) * 4096);
        const unsigned voffV = (unsigned)((sch >> 2) * 1024 + srow * 64 + (sch & 3) * 16);
        const unsigned ldsw = (unsigned)(4 * a0 * 256);
#define STAGE_K(t_) do { _Pragma("unroll") for (int j_ = 0; j_ < 8; ++j_) blds16c(rK, lds + ((t_) & 1) * UNIT + ldsw + j_ * 4096, voffK, hb + (unsigned)(t_) * 32768u + (j_ >> 1) * 8192 + (j_ & 1) * 512); } while (0)
#define STAGE_V(t_) do { const unsigned vo_ = ((t_) == 0 || (t_) == 16) ? (voffV & ~2048u) : voffV, so_ = hb + ((t_) == 0 ? 0u : (unsigned)(t_) * 4096u - 2048u); \
        _Pragma("unroll") for (int j_ = 0; j_ < 8; ++j_) blds16c(rV, lds + ((t_) & 1) * UNIT + VOFF + ldsw + j_ * 4096, vo_, so_ + j_ * 65536); } while (0)
#define KVHOOK(t_) do { if ((t_) < 2) { \
            _Pragma("unroll") for (int i_ = 0; i_ < 4; ++i_) { const u32x4 v_ = *(const LAS u32x4*)(lds + XTRA + ((4 * (t_) + wq) * 4 + i_) * 1024 + lane * 16); \
                bstore128c(v_, rV, (unsigned)((lane & 15) * 64 + (lane >> 4) * 16), (unsigned)(((h * 8 + 2 * wq + (i_ & 1)) * 64 + pr * 4 + (t_) * 2 + 32 + (i_ >> 1)) * 1024)); } } \
        if ((t_) == 2) { if (lane == 0) flag_set(flags + F_KV + h * 32 + pr * 4 + wq, tag); } } while (0)
#define KVCHECK(t_) do { if ((t_) == 6) flag_wait(flags + F_KV + h * 32 + (lane & 31), tag, true); } while (0)
#define SYNC_S(t_) do { KVCHECK(t_); wait_v<0>(); WAIT_L0(); BAR(); asm volatile("" ::: "memory"); if ((t_) + 2 <= qL) STAGE_K((t_) + 2); if ((t_) + 2 <= qL + 1) STAGE_V((t_) + 2); KVHOOK(t_); } while (0)
#define STAGE_KZ(t_) do { _Pragma("unroll") for (int j_ = 0; j_ < 8; ++j_) { blds16c(rK, lds + ((t_) & 1) * UNIT + ldsw + j_ * 4096, voffK, hb + (unsigned)(t_) * 32768u + (j_ >> 1) * 8192 + (j_ & 1) * 512); __builtin_amdgcn_s_sleep(1); } } while (0)
#define STAGE_VZ(t_) do { const unsigned vo_ = ((t_) == 0 || (t_) == 16) ? (voffV & ~2048u) : voffV, so_ = hb + ((t_) == 0 ? 0u : (unsigned)(t_) * 4096u - 2048u); \
        _Pragma("unroll") for (int j_ = 0; j_ < 8; ++j_) { blds16c(rV, lds + ((t_) & 1) * UNIT + VOFF + ldsw + j_ * 4096, vo_, so_ + j_ * 65536); __builtin_amdgcn_s_sleep(1); } } while (0)
#define SYNC_Z(t_) do { KVCHECK(t_); wait_v<0>(); WAIT_L0(); BAR(); asm volatile("" ::: "memory"); if ((t_) + 2 <= qL) STAGE_KZ((t_) + 2); if ((t_) + 2 <= qL + 1) STAGE_VZ((t_) + 2); KVHOOK(t_); } while (0)
#define SYNC_N(t_, n_) do { KVCHECK(t_); wait_v<n_>(); WAIT_L0(); BAR(); asm volatile("" ::: "memory"); if ((t_) + 2 <= qL) STAGE_K((t_) + 2); if ((t_) + 2 <= qL + 1) STAGE_V((t_) + 2); KVHOOK(t_); } while (0)
#define SYNC(t_) do { if (__builtin_expect(grp == 1, 0)) SYNC_S(t_); else { BAR(); asm volatile("" ::: "memory"); } \
        _Pragma("unroll") for (int i_ = 0; i_ < 8; ++i_) c.ka[i_] ^= (unsigned)UNIT; } while (0)
        asm volatile("s_waitcnt vmcnt(0)" : "+v"(kv0) :: "memory");
        if (!__all(kv0 == tag)) flag_wait(flags + F_KVA + h * 64 + lane, tag, true);
        if (grp == 0) { STAGE_K(0); } else { STAGE_V(0); STAGE_K(1); }
#pragma unroll
        for (int d = 0; d < 8; ++d) c.ka[d] = (lbase + (unsigned)(r32 * 256 + ((hi ^ (r32 & 15)) << 4))) ^ (unsigned)(d << 5);
        WAIT_L0(); BAR(); asm volatile("" ::: "memory");
#pragma unroll
        for (int d = 0; d < 8; ++d) c.qr[d] = lds_b128((const LAS unsigned char*)(opaque(c.ka[d] + 98304 + grp * 32768 + wq * 8192)));
        c.dq = 32 * wq + r32 - 4 * hi;
#pragma unroll
        for (int r = 0; r < 16; ++r) { c.o[0][r] = 0.f; c.o[1][r] = 0.f; c.o[2][r] = 0.f; c.o[3][r] = 0.f; c.negm[r] = 0.f; c.s[1][r] = -__builtin_inff(); }
        c.l = 0.f; c.ts = 0.f;
        if (grp == 0) wait_v<0>(); else wait_v<8>();
        WAIT_L0(); BAR(); asm volatile("" ::: "memory");
        if (grp == 1) STAGE_V(1);
#pragma unroll
        for (int i = 0; i < 4; ++i) *(LAS u32x4*)(lds + XTRA + (wid * 4 + i) * 1024 + lane * 16) = dk[i];
        if (grp == 0) __builtin_amdgcn_s_setprio(1); else __builtin_amdgcn_s_setprio(3);
        int t = 0;
        auto nosync = [] {};
        auto sync = [&] { SYNC(t); ++t; };
        halfstep<0, true, true>(c, qb == 0 && wq == 0, nosync);
        halfstep<1, true, false>(c, qb == 0 && wq <= 1, nosync);
        for (; t < qb;) {
            halfstep<2, false, false>(c, false, nosync);
            halfstep<3, false, false>(c, false, sync);
            const bool last = t == qb;
            halfstep<0, false, false>(c, last && wq == 0, nosync);
            halfstep<1, false, false>(c, last && wq <= 1, nosync);
        }
        if (wq >= 2) {
            halfstep<2, false, false>(c, wq == 2, nosync);
            halfstep<3, false, false>(c, true, sync);
        }
        { const int pos = wq >= 2 ? 0 : 2;
          pv_tail(c, c.p[0], c.ka[0], pos);
          finish_last(c);
          pv_tail(c, c.p[1], c.ka[0], pos + 1); }
        { const float rl = 1.0f / swap_add(c.l);
          const rsrc_t rC = mk_rsrc(ws + WS_CTX, (unsigned)TSZ);
          const unsigned so = (unsigned)(((qb * 4 + wq) * 128 + h * 4) * 2048), vo = (unsigned)((r32 & 15) * 64 + ((r32 >> 4) * 2 + hi) * 16);
#pragma unroll
          for (int b = 0; b < 4; ++b) {
              u32x4 A, B;
#pragma unroll
              for (int i = 0; i < 4; ++i) {
                  const unsigned xw = cvtpk(c.o[b][2 * i] * rl, c.o[b][2 * i + 1] * rl), yw = cvtpk(c.o[b][8 + 2 * i] * rl, c.o[b][9 + 2 * i] * rl);
                  const auto r = __builtin_amdgcn_permlane16_swap(xw, yw, false, false); A[i] = r[0]; B[i] = r[1];
              }
              bstore128c(A, rC, vo, so + 2048 * b); bstore128c(B, rC, vo, so + 2048 * b + 1024);
          }
        }
        u64* const fctx = flags + F_CTX + (qb * 32 + h) * 4 + wq;
        const int task = (6 - pr) * 32 + h; const bool hostwg = pr <= 6 && task < 152, host = hostwg && grp == 1;
        const int cc = lane & 15, a = 4 * wq + (lane >> 4), blk = (task & 7) * 19 + (task >> 3);
        const unsigned tvo = (unsigned)(a * 128 + cc * 8), tso = (unsigned)(blk * 32768);
        const rsrc_t rWP = mk_rsrc(ws + WS_WP, fb::TBYTES / 2);
        fb::cf xx[16], tw[16]; unsigned wpk[16];
        auto b2_load = [&] {
            flag_waitN<12>(flags + F_B1, flags + F_WP - 256, tag, lane);
#pragma unroll
            for (int b = 0; b < 16; ++b) xx[b] = fb::ldc(rT1, tvo, tso + b * 2048);
#pragma unroll
            for (int q = 0; q < 16; ++q) wpk[q] = __builtin_amdgcn_raw_buffer_load_b32(rWP, tvo >> 1, (tso >> 1) + q * 1024, 16);
#pragma unroll
            for (int p = 1; p < 16; ++p) { tw[p] = fb::twid((float)(a * p) * (1.0f / 256.0f)); PIN(tw[p]); }
        };
        __builtin_amdgcn_s_setprio(0);
        if (grp == 0) {
            for (; t <= qL; ++t) BAR();
            if (hostwg) { BAR(); wait_v<0>(); if (lane == 0) flag_set(fctx, tag); }
            else pub = fctx;
        } else {
            if (t <= qL) { SYNC_N(t, 8); ++t; }
            wait_v<0>();
            if (lane == 0) flag_set(fctx, tag);
            unsigned d0;
            { const rsrc_t rWo = mk_rsrc(wo, NQKV * DM * 2); const int e = wq * 64 + lane;
              asm volatile("buffer_load_dword %0, %1, %2, %3 offen" : "=v"(d0) : "v"((unsigned)(e < 144 ? e : 143) * 128u), "s"(rWo), "s"((unsigned)((xcd * 512 + idx * 16) * DM * 2)) : "memory");
              if (t > qL) wait_v<0>(); }
            if (pr < 4) {
                const int i = pr * 32 + h, tt = tid - 256, jj = tt >> 5, uu = (tt >> 4) & 1;
                const u16* wrp = wr_ + (16 * i + 8 * uu + jj) * 289; const u16* wip = wi_ + (16 * i + 8 * uu + jj) * 289;
                u16 fr_[19], fi_[19];
#pragma unroll
                for (int g = 0; g < 19; ++g) { const int k = g < 18 ? 16 * g + cc : 288; fr_[g] = wrp[k]; fi_[g] = wip[k]; }
                SYNC_N(t, 38); ++t;
                unsigned pk_[19];
#pragma unroll
                for (int g = 0; g < 19; ++g) { pk_[g] = (unsigned)fr_[g] | ((unsigned)fi_[g] << 16); PIN(pk_[g]); }
#pragma unroll
                for (int g = 0; g < 19; ++g) __builtin_amdgcn_raw_buffer_store_b32(pk_[g], rWP, (unsigned)(jj * (19 * 16384) + uu * 64 + cc * 4), (unsigned)(g * 16384 + i * 128), 16);
                SYNC_N(t, 19); ++t;
                wait_v<0>();
                if (lane == 0) flag_set(flags + F_WP + i * 4 + wq, tag);
            }
            if (host) {
                using namespace fb;
                for (; t < qL - 2; ++t) SYNC_Z(t);
                for (; t < qL - 1; ++t) SYNC_S(t);
                b2_load();
                if (task == 0 && tid == 256) flag_set(flags + F_EPOCH, tag);
                SYNC_N(t, 32); ++t;
                LAS cf* L = (LAS cf*)(lds + XTRA) + cc;
                dft16<-1>(xx);
#pragma unroll
                for (int p = 1; p < 16; ++p) xx[p] = cmulc(xx[p], tw[p]);
#pragma unroll
                for (int p = 0; p < 16; ++p) L[(a * 16 + p) * 16] = xx[p];
                SYNC_S(t); ++t;
#pragma unroll
                for (int i = 0; i < 16; ++i) xx[i] = L[(i * 16 + a) * 16];
                dft16<-1>(xx);
#pragma unroll
                for (int q = 0; q < 16; ++q) xx[q] = cmul(xx[q], (cf){bfl(wpk[q]), bfh(wpk[q])});
                dft16<1>(xx);
#pragma unroll
                for (int i = 1; i < 16; ++i) xx[i] = cmul(xx[i], tw[i]);
#pragma unroll
                for (int i = 0; i < 16; ++i) L[(i * 16 + a) * 16] = xx[i];
                WAIT_L0(); BAR(); asm volatile("" ::: "memory");
#pragma unroll
                for (int p = 0; p < 16; ++p) xx[p] = L[(a * 16 + p) * 16];
                dft16<1>(xx);
#pragma unroll
                for (int b = 0; b < 16; ++b) stc(rT2, tvo, tso + b * 2048, xx[b]);
                pub = flags + F_B2 + task * 4 + wq;
            }
            for (; t <= qL; ++t) SYNC_Z(t);
            asm volatile("" :: "v"(d0));
        }
#undef STAGE_K
#undef STAGE_V
#undef SYNC
#undef SYNC_S
#undef SYNC_N
    }

    {
        using namespace k3;
        St s; s.lds = lds;
        const int lane = lane_id(), tid = wid * 64 + lane, wm = wid & 3, nh = wid >> 2, fr = lane & 15, fq = lane >> 4;
        const int sk = xcd >> 1, nt = idx & 3, mt = (pr >> 2) * 8 + (xcd & 1) * 4 + (pr & 3), tile = mt * 4 + nt;
        s.rA = mk_rsrc(ws + WS_CTX, (unsigned)TSZ); s.rB = mk_rsrc(wo, NQKV * DM * 2);
        s.sa = (unsigned)((mt * 4 * 256 + sk * 64) * 1024); s.sb = (unsigned)(sk * 1024 * DM * 2 + nt * 144 * 2);
        s.extra = wid < 2; s.ldsw = wid * 1024;
#pragma unroll
        for (int i = 0; i < 2; ++i) { const int b = tid * 16 + i * 8192, r = b >> 7, kc = ((b >> 4) & 7) ^ ((r >> 1) & 7); s.voffA[i] = (unsigned)(((r >> 5) * 128 + (kc >> 2)) * 2048 + (r & 31) * 64 + (kc & 3) * 16); }
#pragma unroll
        for (int i = 0; i < 3; ++i) { const int b = tid * 16 + i * 8192, rho = (b / 288) & 63, ch = (b % 288) >> 4;
            const int r5 = rho & 31, pos = (rho & 32) + 8 * ((r5 >> 2) & 3) + 4 * (r5 >> 4) + (r5 & 3);
            const int d = (pos & ~15) + 8 * ((pos >> 2) & 1) + 4 * ((pos >> 3) & 1) + (pos & 3);
            s.voffB[i] = (unsigned)(d * DM * 2 + ch * 16); }
        s.aoff[0] = lbase + (unsigned)((wm * 32 + fr) * 128 + ((fq ^ ((fr >> 1) & 7)) * 16)); s.aoff[1] = s.aoff[0] ^ 64u;
        s.boff = lbase + ABYTES + (unsigned)((4 * fq + ((lane & 15) >> 2)) * 288 + 8 * (lane & 3) + nh * 160);
        const u64* const fin = flags + F_CTX + (mt * 32 + sk * 8) * 4 + (lane & 31);
        const bool early = pr < 4;
        if (early && wid == 7) flag_wait(fin, tag, lane < 32);
        WAIT_L0(); BAR(); asm volatile("" ::: "memory");
        stageB(s, 0, 0); stageB(s, 1, 1); stageB(s, 2, 2); stageB(s, 3, 3);
        if (early) { stageA(s, 0, 0); stageA(s, 1, 1); stageA(s, 2, 2); stageA(s, 3, 3); wait_v<16>(); } else wait_v<8>();
        if (pub != nullptr && lane == 0) flag_set(pub, tag);
        LAS fb::cf* const L3 = (LAS fb::cf*)(lds + NSTG * STG);
        fb::Prep pp; if (wid < 5) fb::prep<false>(pp, L3, s0, tid);
        if (!early) {
            if (wid == 7) flag_wait(fin, tag, lane < 32);
            BAR(); asm volatile("" ::: "memory");
            stageA(s, 0, 0); stageA(s, 1, 1); stageA(s, 2, 2); stageA(s, 3, 3);
        }
        wait_v<6>(); BAR();
        fb::cf z[8];
        u64 pv[2];
        auto late = [&](auto ic) {
            constexpr int I = decltype(ic)::value;
            const u64* fp = flags + F_B2 + wid * 76;
            if constexpr (I == 0) {
                asm volatile("global_load_dwordx2 %0, %1, off sc1" : "=v"(pv[0]) : "v"(fp + lane) : "memory");
                asm volatile("global_load_dwordx2 %0, %1, off sc1" : "=v"(pv[1]) : "v"(fp + 64 + (lane < 12 ? lane : 0)) : "memory");
            } else if constexpr (I == 1) {
                asm volatile("" : "+v"(pv[0]), "+v"(pv[1]));
                if (!__all(pv[0] == tag && pv[1] == tag)) flag_waitN<2>(fp, tag, lane, 76);
            } else fb::b3_load(z, rT2, s0, tid);
        };
        if (nh == 0) mainloop<5>(s, late); else mainloop<4>(s, late);
        const rsrc_t rS = mk_rsrc(ws + WS_SLAB, 64 * 4 * 4 * 20 * SLABQ);
        if (wm != sk) {
            const unsigned so = (unsigned)((((tile * 4 + wm) * 4 + sk) * 20 + nh * 10) * SLABQ);
#pragma unroll
            for (int q = 0; q < 10; ++q) if (q < 8 || nh == 0) bstore128c(__builtin_bit_cast(u32x4, s.acc[q & 1][q >> 1]), rS, lane * 16, so + q * SLABQ);
        } else {
#pragma unroll
            for (int q = 0; q < 10; ++q) if (q < 8 || nh == 0) *(LAS f32x4*)(lds + (nh * 10 + q) * SLABQ + lane * 16) = s.acc[q & 1][q >> 1];
        }
        const u64* const fsl = flags + F_SLAB + (tile * 4 + sk) * 8 + (lane & 7); const bool fon = ((lane & 7) >> 1) != sk;
        fb::b3(pp, z, out, L3, s0, tid, [&](auto ic) {
            if constexpr (decltype(ic)::value == 0) { wait_v<0>(); if (lane == 0 && wm != sk) flag_set(flags + F_SLAB + (tile * 4 + wm) * 8 + sk * 2 + nh, tag); }
            if constexpr (decltype(ic)::value == 1) flag_peek(pv[0], fsl); });
        asm volatile("s_waitcnt vmcnt(0)" : "+v"(pv[0]) :: "memory");
        if (!__all(!fon || pv[0] == tag)) flag_wait(fsl, tag, fon);
        { f32x4 part[3][3];
          const bool has3 = wid < 2;
#pragma unroll
          for (int i = 0; i < 3; ++i) { const int ix = i < 2 || has3 ? wid + 8 * i : wid;
#pragma unroll
              for (int j = 0; j < 3; ++j) { const int src = (sk + 1 + j) & 3;
                  part[j][i] = __builtin_bit_cast(f32x4, __builtin_amdgcn_raw_buffer_load_b128(rS, lane * 16, (unsigned)((((tile * 4 + sk) * 4 + src) * 20 + ix) * SLABQ), 16)); } }
          SCHED(); fb::b3_out(pp, out, s0, tid); SCHED();
          u16* op = out + (size_t)(mt * 128 + sk * 32 + fr) * (2 * DM) + nt * 144 + 4 * fq;
#pragma unroll
          for (int i = 0; i < 3; ++i) if (i < 2 || has3) {
              const int ix = wid + 8 * i, hn = ix >= 10 ? 1 : 0, q = ix - 10 * hn;
              const f32x4 mine = *(const LAS f32x4*)(lds + ix * SLABQ + lane * 16);
              const f32x4 r = (mine + part[0][i]) + (part[1][i] + part[2][i]);
              *(u32x2*)(op + (size_t)(q & 1) * 16 * (2 * DM) + hn * 80 + 16 * (q >> 1)) = (u32x2){cvtpk(r[0], r[1]), cvtpk(r[2], r[3])};
          } }
    }
}

extern "C" void kernel_launch(void* const* d_in, const int* in_sizes, int n_in, void* d_out, int out_size, void* d_ws, size_t ws_size, hipStream_t stream) {
    (void)in_sizes; (void)n_in; (void)out_size; (void)ws_size;
    (void)hipFuncSetAttribute((const void*)fused, hipFuncAttributeMaxDynamicSharedMemorySize, 163840);
    fused<<<256, 512, 163840, stream>>>((const u16*)d_in[0], (const u16*)d_in[1], (const u16*)d_in[2], (const u16*)d_in[3], (const u16*)d_in[4], (const u16*)d_in[5],
                                        (const u16*)d_in[6], (const u16*)d_in[7], (const u16*)d_in[8], (u16*)d_out, (char*)d_ws);
}
```
